```python
import torch

def _softmax(x, axis=-1):
    return torch.softmax(x, dim=axis)
def _gelu(x):
    return torch.nn.functional.gelu(x, approximate="tanh")
def _silu(x):
    return torch.nn.functional.silu(x)
def _sigmoid(x):
    return torch.sigmoid(x)
def _layernorm(x, axis=-1, eps=1e-5):
    m = x.mean(dim=axis, keepdim=True)
    v = x.var(dim=axis, keepdim=True, unbiased=False)
    return (x - m) / torch.sqrt(v + eps)
def _scatter_add(tgt, idx, src, axis=0):
    return tgt.clone().index_add_(axis, idx, src)
def _segment_sum(src, seg, n):
    out = torch.zeros((n,) + src.shape[1:], dtype=src.dtype)
    return out.index_add_(0, seg, src)
def _pad(x, pw):
    flat = tuple(v for pair in reversed(pw) for v in pair)
    return torch.nn.functional.pad(x, flat)

kernel_name = 'standalone__mha_block_cached__near_ridge'

def setup_inputs():

    _w = (lambda a: (lambda t: t.to(torch.bfloat16) if t.is_floating_point() else t)(torch.as_tensor(a)))
    B, Sq, D = 16, 128, 512
    H, Sk, Dh = 32, 512, 128
    return {
        "x": _w((lambda *s: torch.randn(*s, dtype=torch.bfloat16) * 0.8)(B, Sq, D)),
        "k_cache": _w((lambda *s: torch.randn(*s, dtype=torch.bfloat16) * 0.8)(B, H, Sk, Dh)),
        "v_cache": _w((lambda *s: torch.randn(*s, dtype=torch.bfloat16) * 0.05)(B, H, Sk, Dh)),
        "wq": _w((lambda *s: torch.randn(*s, dtype=torch.bfloat16) * 0.8)(D, H * Dh) * (D ** -0.5)),
        "wo": _w((lambda *s: torch.randn(*s, dtype=torch.bfloat16) * 0.05)(H * Dh, D) * ((H * Dh) ** -0.5)),
    }

def reference(x, k_cache, v_cache, wq, wo):
    B, Sq, D = x.shape
    H, Dh = 32, 128
    q = (lambda x, *d: x.permute(*d))((x @ wq).reshape(B, Sq, H, Dh), 0, 2, 1, 3)
    scores = torch.einsum("bhqd,bhkd->bhqk", q, k_cache) * (Dh ** -0.5)
    p = _softmax(scores, axis=-1)
    o = torch.einsum("bhqk,bhkd->bhqd", p, v_cache)
    return (lambda x, *d: x.permute(*d))(o, 0, 2, 1, 3).reshape(B, Sq, H * Dh) @ wo
```

```cpp
#include <hip/hip_runtime.h>
#include <hip/hip_bf16.h>
typedef __hip_bfloat16 bf16;
constexpr int B=16,SQ=128,D=512,H=32,SK=512,DH=128;
__global__ void gemm_naive(const bf16* A,const bf16* Bm,bf16* C,int M,int N,int K){
  __shared__ float As[16][17], Bs[16][17];
  int tx=threadIdx.x, ty=threadIdx.y; int row=blockIdx.y*16+ty, col=blockIdx.x*16+tx; float acc=0;
  for(int k0=0;k0<K;k0+=16){ As[ty][tx]=__bfloat162float(A[(size_t)row*K+k0+tx]); Bs[ty][tx]=__bfloat162float(Bm[(size_t)(k0+ty)*N+col]); __syncthreads();
    for(int k=0;k<16;++k) acc+=As[ty][k]*Bs[k][tx]; __syncthreads(); }
  C[(size_t)row*N+col]=__float2bfloat16(acc);
}
__global__ void attn_naive(const bf16* Q,const bf16* K,const bf16* V,bf16* O){
  int q=blockIdx.x, h=blockIdx.y, b=blockIdx.z, t=threadIdx.x;
  __shared__ float qs[DH], s[SK], red[128];
  qs[t]=__bfloat162float(Q[((size_t)(b*SQ+q))*(H*DH)+h*DH+t]); __syncthreads();
  const bf16* Kb=K+((size_t)(b*H+h))*SK*DH; const bf16* Vb=V+((size_t)(b*H+h))*SK*DH;
  float m=-1e30f;
  for(int k=t;k<SK;k+=128){ float a=0; for(int d=0;d<DH;++d) a+=qs[d]*__bfloat162float(Kb[(size_t)k*DH+d]); a*=0.08838834764831845f; s[k]=a; m=fmaxf(m,a);}
  red[t]=m; __syncthreads(); for(int o=64;o>0;o>>=1){ if(t<o) red[t]=fmaxf(red[t],red[t+o]); __syncthreads(); } m=red[0]; __syncthreads();
  float l=0; for(int k=t;k<SK;k+=128){ float e=__expf(s[k]-m); s[k]=e; l+=e; }
  red[t]=l; __syncthreads(); for(int o=64;o>0;o>>=1){ if(t<o) red[t]+=red[t+o]; __syncthreads(); } l=red[0];
  float a=0; for(int k=0;k<SK;++k) a+=s[k]*__bfloat162float(Vb[(size_t)k*DH+t]);
  O[((size_t)(b*SQ+q))*(H*DH)+h*DH+t]=__float2bfloat16(a/l);
}
extern "C" void kernel_launch(void* const* d_in, const int* in_sizes, int n_in, void* d_out, int out_size, void* d_ws, size_t ws_size, hipStream_t stream) {
  const bf16* x=(const bf16*)d_in[0]; const bf16* k=(const bf16*)d_in[1]; const bf16* v=(const bf16*)d_in[2]; const bf16* wq=(const bf16*)d_in[3]; const bf16* wo=(const bf16*)d_in[4];
  bf16* Q=(bf16*)d_ws; bf16* O=Q+(size_t)B*SQ*H*DH;
  gemm_naive<<<dim3(H*DH/16,B*SQ/16),dim3(16,16),0,stream>>>(x,wq,Q,B*SQ,H*DH,D);
  attn_naive<<<dim3(SQ,H,B),128,0,stream>>>(Q,k,v,O);
  gemm_naive<<<dim3(D/16,B*SQ/16),dim3(16,16),0,stream>>>(O,wo,(bf16*)d_out,B*SQ,D,H*DH);
}
```

```cpp
#include <hip/hip_runtime.h>
#include <hip/hip_bf16.h>

typedef short    bf16x8 __attribute__((ext_vector_type(8)));
typedef short    s16x4  __attribute__((ext_vector_type(4)));
typedef float    f32x16 __attribute__((ext_vector_type(16)));
typedef float    f32x4  __attribute__((ext_vector_type(4)));
typedef float    f32x2  __attribute__((ext_vector_type(2)));
typedef __bf16   bf16x2 __attribute__((ext_vector_type(2)));
typedef unsigned u32x4  __attribute__((ext_vector_type(4)));
typedef unsigned u32x2  __attribute__((ext_vector_type(2)));
typedef unsigned long long u64;
typedef __amdgpu_buffer_rsrc_t rsrc_t;
#define LAS __attribute__((address_space(3)))
#define GAS __attribute__((address_space(1)))
#define DI __device__ __forceinline__
#define MFMA(a, b, c) __builtin_amdgcn_mfma_f32_32x32x16_bf16((a), (b), (c), 0, 0, 0)
#define MFMA16(a, b, c) __builtin_amdgcn_mfma_f32_16x16x32_bf16((a), (b), (c), 0, 0, 0)

constexpr float QSCALE = 0.08838834764831845f * 1.4426950408889634f;
constexpr float THR = 8.0f;

constexpr unsigned SLOT = 16384, PAIR = 32768, RING = 4 * PAIR;
constexpr unsigned L_PARK = RING, L_FLAG = L_PARK + 4 * 7168, L_END = L_FLAG + 64;
constexpr size_t WS_O = 0;
constexpr size_t WS_P = 16u << 20;
constexpr size_t WS_F = 24u << 20;
constexpr size_t WS_E = 25u << 20;

#define STAMP(i)
#define STAMPV(i, v)
#define STAMP_DECL
#define STAMP_DUMP()
#define CYC() 0ull

DI unsigned cvtpk(float lo, float hi) { f32x2 v = {lo, hi}; bf16x2 b = __builtin_convertvector(v, bf16x2); return __builtin_bit_cast(unsigned, b); }
DI bf16x8 pack8(const f32x16& x, int s, float sc) {
  u32x4 w = {cvtpk(x[8 * s] * sc, x[8 * s + 1] * sc), cvtpk(x[8 * s + 2] * sc, x[8 * s + 3] * sc), cvtpk(x[8 * s + 4] * sc, x[8 * s + 5] * sc), cvtpk(x[8 * s + 6] * sc, x[8 * s + 7] * sc)};
  return __builtin_bit_cast(bf16x8, w);
}
DI u32x4 pack8n(const f32x16& x, int s) {
  u32x4 w = {cvtpk(x[8 * s], x[8 * s + 1]), cvtpk(x[8 * s + 2], x[8 * s + 3]), cvtpk(x[8 * s + 4], x[8 * s + 5]), cvtpk(x[8 * s + 6], x[8 * s + 7])};
  return w;
}
DI bf16x8 lds_b128(unsigned a) { return *(const LAS bf16x8*)(size_t)a; }
DI s16x4 lds_tr(unsigned a) { return __builtin_amdgcn_ds_read_tr16_b64_v4i16((LAS s16x4*)(size_t)a); }
DI bf16x8 lds_tr2(unsigned a0, unsigned a1) { s16x4 lo = lds_tr(a0), hi = lds_tr(a1); return __builtin_shufflevector(lo, hi, 0, 1, 2, 3, 4, 5, 6, 7); }

template <int N> DI void waitvm() { asm volatile("s_waitcnt vmcnt(%0)" ::"i"(N) : "memory"); }
DI void dma4_nt(rsrc_t r, unsigned v0, unsigned v1, unsigned v2, unsigned v3, unsigned soff, unsigned m0v) {
  asm volatile("s_mov_b32 m0, %0\n\ts_nop 0\n\t"
               "buffer_load_dwordx4 %1, %5, %6 offen nt lds\n\t"
               "buffer_load_dwordx4 %2, %5, %6 offen offset:1024 nt lds\n\t"
               "buffer_load_dwordx4 %3, %5, %6 offen offset:2048 nt lds\n\t"
               "buffer_load_dwordx4 %4, %5, %6 offen offset:3072 nt lds" ::"s"(m0v), "v"(v0), "v"(v1), "v"(v2), "v"(v3), "s"(r), "s"(soff) : "memory");
}
DI void dma4(rsrc_t r, unsigned v0, unsigned v1, unsigned v2, unsigned v3, unsigned soff, unsigned m0v) {
  asm volatile("s_mov_b32 m0, %0\n\ts_nop 0\n\t"
               "buffer_load_dwordx4 %1, %5, %6 offen lds\n\t"
               "buffer_load_dwordx4 %2, %5, %6 offen offset:1024 lds\n\t"
               "buffer_load_dwordx4 %3, %5, %6 offen offset:2048 lds\n\t"
               "buffer_load_dwordx4 %4, %5, %6 offen offset:3072 lds" ::"s"(m0v), "v"(v0), "v"(v1), "v"(v2), "v"(v3), "s"(r), "s"(soff) : "memory");
}
DI unsigned flag_rd(unsigned a) { unsigned v; asm volatile("ds_read_b32 %0, %1\n\ts_waitcnt lgkmcnt(0)" : "=v"(v) : "v"(a) : "memory"); return __builtin_amdgcn_readfirstlane(v); }
DI unsigned flag_rd_min2(unsigned a) { u32x2 v; asm volatile("ds_read_b64 %0, %1\n\ts_waitcnt lgkmcnt(0)" : "=v"(v) : "v"(a) : "memory"); return __builtin_amdgcn_readfirstlane(min(v[0], v[1])); }
DI unsigned flag_rd_min4(unsigned a) { unsigned v; asm volatile("ds_read_b32 %0, %1 offset:8\n\ts_waitcnt lgkmcnt(0)" : "=v"(v) : "v"(a) : "memory");
  return min(min((unsigned)__builtin_amdgcn_readlane(v, 0), (unsigned)__builtin_amdgcn_readlane(v, 1)), min((unsigned)__builtin_amdgcn_readlane(v, 2), (unsigned)__builtin_amdgcn_readlane(v, 3))); }
DI void free_wr(unsigned a, unsigned v) { asm volatile("ds_write_b32 %0, %1 offset:8" ::"v"(a), "v"(v) : "memory"); }
DI void flag_wr(unsigned a, unsigned v) { asm volatile("ds_write_b32 %0, %1" ::"v"(a), "v"(v) : "memory"); }
constexpr unsigned SPIN_MAX = 1u << 18, GSPIN_MAX = 1u << 15;
DI void wait_ge(unsigned a, unsigned need) {
#pragma unroll 1
  for (unsigned s = 0; s < SPIN_MAX; ++s) { if (flag_rd(a) >= need) break; __builtin_amdgcn_s_sleep(1); }
}
DI void wait_ge2(unsigned a, unsigned need) {
#pragma unroll 1
  for (unsigned s = 0; s < SPIN_MAX; ++s) { if (flag_rd_min2(a) >= need) break; __builtin_amdgcn_s_sleep(1); }
}
DI u32x2 flag_peek2(unsigned a) { return *(volatile LAS u32x2*)(size_t)a; }
#define SBAR() __builtin_amdgcn_sched_barrier(0)
#define PIN(x) asm volatile("" : "+v"(x))
DI float max3f(float a, float b, float c) { return __builtin_fmaxf(__builtin_fmaxf(a, b), c); }
DI void add_col3(f32x16& c0, f32x16& c1, f32x16& c2, float aone, float v) {
  asm volatile("s_nop 1\n\tv_mfma_f32_32x32x2_f32 %0, %3, %4, %0\n\tv_mfma_f32_32x32x2_f32 %1, %3, %4, %1\n\tv_mfma_f32_32x32x2_f32 %2, %3, %4, %2\n\ts_nop 15\n\ts_nop 3"
               : "+v"(c0), "+v"(c1), "+v"(c2) : "v"(aone), "v"(v));
}
DI unsigned lane_id() { unsigned l; asm volatile("v_mbcnt_lo_u32_b32 %0, -1, 0\n\tv_mbcnt_hi_u32_b32 %0, -1, %0" : "=v"(l)); return l; }
DI f32x16 zero16() { f32x16 z;
#pragma unroll
  for (int i = 0; i < 16; ++i) { float t; asm volatile("v_mov_b32 %0, 0" : "=v"(t)); z[i] = t; }
  return z; }
#define EXP2(x) __builtin_amdgcn_exp2f(x)
DI u64 gld(const char* p) { return __hip_atomic_load((const GAS u64*)p, __ATOMIC_RELAXED, __HIP_MEMORY_SCOPE_AGENT); }
DI void gst(char* p, u64 v) { __hip_atomic_store((GAS u64*)p, v, __ATOMIC_RELAXED, __HIP_MEMORY_SCOPE_AGENT); }
DI void gwait(const char* p, u64 tag) {
#pragma unroll 1
  for (unsigned s = 0; s < GSPIN_MAX; ++s) { if (__all(gld(p) == tag)) break; __builtin_amdgcn_s_sleep(2); }
  asm volatile("" ::: "memory");
}
constexpr int SC1 = 16;

__global__ __launch_bounds__(384, 2) void mha_fused(const char* __restrict__ X, const char* __restrict__ K, const char* __restrict__ V,
                                                    const char* __restrict__ WQ, const char* __restrict__ WO, char* WS, char* __restrict__ OUT
) {
  extern __shared__ __attribute__((aligned(16))) char shm[];
  const unsigned lds0 = (unsigned)(size_t)(LAS char*)shm;
  const int tid = threadIdx.x, lane = tid & 63;
  const int wid = __builtin_amdgcn_readfirstlane(tid >> 6);
  const int g = blockIdx.x & 7, idx = blockIdx.x >> 3;
  const int bl = idx & 7, b = 8 * (g & 1) + bl, gj = g >> 1, gr = idx >> 3, hp = 4 * gj + gr;
  const unsigned fl = lds0 + L_FLAG;
  STAMP_DECL;
  if (wid >= 4) {
    const int L = wid - 4;
    __builtin_amdgcn_s_setprio(3);
    const rsrc_t rx = __builtin_amdgcn_make_buffer_rsrc((void*)(X + (size_t)b * 131072), 0, 131072, 0x00020000);
    unsigned vx[4];
#pragma unroll
    for (int j = 0; j < 4; ++j) vx[j] = (lane >> 3) * 1024 + (((lane & 7) ^ ((4 * j + (lane >> 4)) & 7)) * 16) + j * (8192 - 1024);
    const unsigned d0 = (unsigned)(gr + (bl >> 1)), sub = (unsigned)bl & 1u;
    unsigned n = 0, nfree = 0, pub = 0, m = lds0 + L * SLOT;
    unsigned fcyc = 1; (void)fcyc;
    const unsigned blkfl = fl + (lane & 3) * 16, myfl = blkfl + L * 4;
#define PUB(v) do { if ((int)(v) > (int)pub) { pub = (v); flag_wr(myfl, pub); } } while (0)
    auto need_free = [&]() {
      const int need = (int)n - 3;
      if ((int)nfree < need) {
        const unsigned f0_ = (unsigned)CYC();
        nfree = flag_rd_min4(blkfl);
        if ((int)nfree < need) {
          waitvm<0>(); PUB(n);
#pragma unroll 1
          for (unsigned s_ = 0; s_ < SPIN_MAX; ++s_) { nfree = flag_rd_min4(blkfl); if ((int)nfree >= need) break; __builtin_amdgcn_s_sleep(1); }
        }
        fcyc += (unsigned)CYC() - f0_;
      }
    };
#define LD_ITEM(ISSUE)                                                                     \
    do {                                                                                   \
      need_free();                                                                         \
      waitvm<12>(); ISSUE(0); waitvm<12>(); ISSUE(1); waitvm<12>(); ISSUE(2);              \
      waitvm<12>(); PUB(n);                                                                \
      ISSUE(3); ++n;                                                                       \
      m = lds0 + L * SLOT + (n & 3) * PAIR;                                                \
    } while (0)
#define ISS_X(j) dma4(rx, vx[0], vx[1], vx[2], vx[3], so + (j) * 32768u, m + (j) * 4096u)
#define ISS_W(j) dma4(rw, vw[0], vw[1], vw[2], vw[3], so + (j) * 131072u, m + (j) * 4096u)
#define ISS_O(j) dma4(ro, vo[0], vo[1], vo[2], vo[3], so + (j) * 16384u, m + (j) * 4096u)
#define ISS_K(j) dma4_nt(rk, vk[0], vk[1], vk[2], vk[3], so + (j) * 4096u, m + (j) * 4096u)
#define ISS_V(j) dma4_nt(rv, vv, vv, vv, vv, so + (j) * 4096u, m + (j) * 4096u)
    { const unsigned so = (2u * (d0 & 3u) + ((unsigned)L ^ sub)) * 128u; ISS_X(0); ISS_X(1); ISS_X(2); ISS_X(3); n = 1; m += PAIR; }
    STAMP(0);
    if (L == 0 && lane < 16) *(LAS unsigned*)(size_t)(fl + lane * 4) = 0u;
    asm volatile("s_waitcnt lgkmcnt(0)\n\ts_barrier" ::: "memory");
    const unsigned t4 = (lane >> 4) * 64 + (lane & 3) * 16, r4 = (lane >> 2) & 3;
    {
      const rsrc_t rw = __builtin_amdgcn_make_buffer_rsrc((void*)(WQ + (size_t)hp * 512 + L * 256), 0, 4194304 - hp * 512 - L * 256, 0x00020000);
      unsigned vw[4];
#pragma unroll
      for (int j = 0; j < 4; ++j) vw[j] = r4 * 8192 + t4 + j * (32768 - 1024);
#pragma unroll 1
      for (unsigned D = 0; D < 4; ++D) {
        const unsigned dd = 2u * ((D + d0) & 3u);
        if (D) { const unsigned so = (dd + ((unsigned)L ^ sub)) * 128u; LD_ITEM(ISS_X); }
#pragma unroll 1
        for (unsigned u = 0; u < 2; ++u) { const unsigned so = (dd + (u ^ sub)) * 524288u; LD_ITEM(ISS_W); }
      }
    }
    STAMP(8); STAMPV(22, fcyc); fcyc = 1;
    {
      const size_t kvb = ((size_t)b * 16 + hp) * 262144;
      const rsrc_t rk = __builtin_amdgcn_make_buffer_rsrc((void*)(K + kvb - 896), 0, 262144 + 896, 0x00020000);
      const rsrc_t rv = __builtin_amdgcn_make_buffer_rsrc((void*)(V + kvb), 0, 262144, 0x00020000);
      unsigned vk[4];
#pragma unroll
      for (int j = 0; j < 4; ++j)
        vk[j] = ((lane >> 2) & 7) * 256 + (lane >> 5) * 64 + (((lane & 3) ^ ((2 * (j >> 1) + ((lane >> 4) & 1)) & 3)) * 16) + ((j & 1) ? 0 : 896);
      const unsigned vv = r4 * 256 + t4;
#pragma unroll 1
      for (int pp = 0; pp < 16; ++pp) {
        const int p = pp & 7;
        const bool isk = L == 0 ? (p == 0) : (p < 7);
        const int tile = L == 0 ? (p == 0 ? 0 : p - 1) : (p < 7 ? p + 1 : 7);
        const unsigned so = ((pp & 8) + tile) * SLOT;
        if (isk) LD_ITEM(ISS_K); else LD_ITEM(ISS_V);
        if (pp == 7) { STAMP(12); }
      }
    }
    STAMP(11); STAMPV(24, fcyc);
    {
      const rsrc_t ro = __builtin_amdgcn_make_buffer_rsrc((void*)(WO + (size_t)gj * 1048576 + gr * 256 + L * 65536), 0, 1048576 - gr * 256 - L * 65536, 0x00020000);
      unsigned vo[4];
#pragma unroll
      for (int j = 0; j < 4; ++j) vo[j] = r4 * 1024 + (((lane >> 4) + gj) & 3) * 64 + (lane & 3) * 16 + j * (4096 - 1024);
#pragma unroll 1
      for (int s = 0; s < 8; ++s) {
        const int hl = s == 0 ? 2 * gr + 1 : (s < 5 ? 2 * ((gr + s - 1) & 3) : 2 * ((gr + s - 4) & 3) + 1);
        const unsigned so = (unsigned)hl * 131072u;
        LD_ITEM(ISS_O);
      }
    }
    waitvm<0>(); PUB(n);
    STAMP(1); STAMP_DUMP();
    return;
  }
  STAMP(0);
  const int r32 = lane & 31, hi = lane >> 5;
  unsigned vfl = fl + wid * 16;
  PIN(vfl);
  const unsigned kbase = lds0 + (r32 >> 3) * 2048 + (r32 & 7) * 64 + ((hi ^ ((r32 >> 2) & 3)) * 16);
  const unsigned vbase = lds0 + hi * 1024 + ((lane >> 2) & 3) * 64 + ((lane >> 4) & 1) * 32 + (lane & 3) * 8;
  asm volatile("s_barrier" ::: "memory");
  u64 tag;
  { const u64 e = gld(WS + WS_E + b * 128) + 1ull;
    tag = ((u64)(unsigned)__builtin_amdgcn_readfirstlane((unsigned)(e >> 32)) << 32) | (unsigned)__builtin_amdgcn_readfirstlane((unsigned)e); }
  const unsigned long long cy0 = CYC(); unsigned long long cyl = cy0; (void)cyl; unsigned wcyc = 1, wcnt = 1; (void)wcyc; (void)wcnt;
#define KRD(a0, a1, OFF, i) lds_b128(((((i) >> 1) & 1) ? (a1) : (a0)) + ((OFF) + 512 * ((i) >> 2) + ((i) & 1) * 8192))
#define VRD(a, OFF, i) lds_tr2((a) + ((OFF) + 4096 * ((i) >> 2) + 256 * ((i) & 3)), (a) + ((OFF) + 4096 * ((i) >> 2) + 256 * ((i) & 3) + 2048))
#define WRD(a, i) lds_tr2((a) + (4096 * ((i) >> 2) + 256 * ((i) & 3)), (a) + (4096 * ((i) >> 2) + 256 * ((i) & 3) + 1024))
  unsigned it = 0;
#define POFF(p) (((p) & 3u) * PAIR)
#define RNEXT(a) (((a) + PAIR) & (RING - 1))
#define SLOW(X) X
#define PEEK() flag_peek2(vfl)
#define CHECK2(pk, need) do { if (__builtin_expect((unsigned)__builtin_amdgcn_readfirstlane(min(pk[0], pk[1])) < (need), 0)) SLOW(wait_ge2(vfl, need)); asm volatile("" ::: "memory"); } while (0)
#define CHECK1(pk, w, need) do { if (__builtin_expect((unsigned)__builtin_amdgcn_readfirstlane(pk[w]) < (need), 0)) SLOW(wait_ge(vfl + 4 * (w), need)); asm volatile("" ::: "memory"); } while (0)
#define FREE(v) free_wr(vfl, v)
  bf16x8 qf[8], qkeep, fr[36];
  unsigned ca, ka0, ka1;
  {
    const unsigned wbase = lds0 + hi * 2048 + ((lane >> 2) & 3) * 64 + ((lane >> 4) & 1) * 32 + (((lane & 1) << 1) | ((lane >> 1) & 1)) * 8;
    const unsigned xbase = lds0 + (wid * 32 + r32) * 128 + ((hi ^ ((r32 >> 1) & 7)) * 16);
    f32x16 acc[8];
#pragma unroll
    for (int d = 0; d < 8; ++d) acc[d] = f32x16{};
    bf16x8 xf[8];
    SLOW(wait_ge2(vfl, 1));
#pragma unroll
    for (int s = 0; s < 8; ++s) xf[s] = lds_b128((xbase ^ (32 * (s & 3))) + (s >> 2) * SLOT);
    asm volatile("" : "+s"(tag));
    FREE(1);
    SLOW(wait_ge2(vfl, 2));
    STAMP(9);
    ca = wbase + PAIR; it = 2;
    fr[1] = WRD(ca, 1); fr[0] = WRD(ca, 0); fr[3] = WRD(ca, 3); fr[2] = WRD(ca, 2);
#define QPAIR(U, NEXT)                                                                                                  \
    do { u32x2 pk = {0u, 0u};                                                                                           \
      SBAR();                                                                                                           \
      _Pragma("unroll") for (int i = 0; i < 32; ++i) {                                                                  \
        acc[(i >> 4) * 4 + (i & 3)] = MFMA(fr[i], xf[(U) * 4 + ((i >> 2) & 3)], acc[(i >> 4) * 4 + (i & 3)]);           \
        if (i == 19) pk = PEEK();                                                                                       \
        if (i == 23) { CHECK2(pk, it + ((NEXT) == 1 ? 2 : 1));                                                          \
          if ((NEXT) == 1) { xa = xbase + POFF(it); PIN(xa); ++it; xf[1] = lds_b128(xa ^ 32); xf[0] = lds_b128(xa); } } \
        if ((NEXT) == 1 && i == 25) { xf[3] = lds_b128(xa ^ 96); xf[2] = lds_b128(xa ^ 64); xf[4] = lds_b128(xa + SLOT); xf[5] = lds_b128((xa ^ 32) + SLOT); } \
        if (i == 27) { if ((NEXT) != 1) FREE(it); else FREE(it - 1);                                                    \
          if ((NEXT) == 2) { ka0 = kbase + POFF(it); ka1 = ka0 ^ 32; PIN(ka0); PIN(ka1); } else { ca = wbase + POFF(it); PIN(ca); } ++it; } \
        if ((NEXT) == 1 && i == 29) xf[6] = lds_b128((xa ^ 64) + SLOT);                                                 \
        if ((i & 1) == 0) { const int j = i + 4;                                                                        \
          if (j < 32) { fr[j + 1] = WRD(ca, j + 1); fr[j] = WRD(ca, j); }                                               \
          else if ((NEXT) != 2) { fr[j + 1] = WRD(ca, j - 31); fr[j] = WRD(ca, j - 32); }                               \
          else { fr[j + 1] = KRD(ka0, ka1, 0, j - 31); fr[j] = KRD(ka0, ka1, 0, j - 32); } }                            \
        SBAR();                                                                                                         \
      }                                                                                                                 \
      if ((NEXT) == 1) { xf[7] = lds_b128((xa ^ 96) + SLOT); FREE(it - 1); }                                            \
      _Pragma("unroll") for (int i = 0; i < 4; ++i) fr[i] = fr[32 + i];                                                 \
    } while (0)
    unsigned xa = 0;
#pragma unroll 1
    for (int D = 0; D < 3; ++D) { QPAIR(0, 0); QPAIR(1, 1); }
    QPAIR(0, 0); QPAIR(1, 2);
#pragma unroll
    for (int d = 0; d < 4; ++d) { qf[2 * d] = pack8(acc[d], 0, QSCALE); qf[2 * d + 1] = pack8(acc[d], 1, QSCALE); }
    const unsigned pa = lds0 + L_PARK + wid * 7168 + lane_id() * 16;
#pragma unroll
    for (int i = 0; i < 7; ++i) *(LAS bf16x8*)(size_t)(pa + i * 1024) = pack8(acc[4 + (i >> 1)], i & 1, QSCALE);
    qkeep = pack8(acc[7], 1, QSCALE);
    ca = vbase + POFF(it - 1);
  }
  STAMP(2); STAMPV(16, CYC() - cy0); STAMPV(18, wcyc); STAMPV(19, wcnt); wcyc = 1; wcnt = 1;
  bf16x8 sel;
  { const unsigned l_ = lane_id(); const short one = (((l_ >> 4) ^ (l_ >> 2)) & 1) ? (short)0 : (short)0x3F80;
#pragma unroll
    for (int i = 0; i < 8; ++i) sel[i] = one; }
  const rsrc_t rso = __builtin_amdgcn_make_buffer_rsrc((void*)(WS + WS_O + ((size_t)b * 32 + gj * 8) * 32768 + wid * 8192), 0, 8 * 32768 - wid * 8192, 0x00020000);
  char* const fbase = WS + WS_F + (size_t)b * 16 * 768 + wid * 64;
  bf16x8 of2[8];
#pragma unroll 1
  for (int hd = 0; hd < 2; ++hd) {
    if (hd == 1) {
      const unsigned pa = lds0 + L_PARK + wid * 7168 + lane_id() * 16;
#pragma unroll
      for (int i = 0; i < 7; ++i) qf[i] = lds_b128(pa + i * 1024);
      qf[7] = qkeep;
    }
    f32x16 o[4], sa[2], sb[2], negm;
    f32x4 ls = {0.f, 0.f, 0.f, 0.f};
    u32x4 pw[4];
    float fct = 1.f;
    bool pend = false;
    { const f32x16 zero = {};
      SBAR();
#pragma unroll
      for (int i = 0; i < 16; ++i) {
        sa[i & 1] = MFMA(fr[i], qf[i >> 1], i < 2 ? zero : sa[i & 1]);
        if ((i & 1) == 0) { const int j = i + 4;
          if (j < 16) { fr[j + 1] = KRD(ka0, ka1, 0, j + 1); fr[j] = KRD(ka0, ka1, 0, j); } else { fr[j + 1] = KRD(ka0, ka1, SLOT, j - 15); fr[j] = KRD(ka0, ka1, SLOT, j - 16); } }
        if ((i & 3) == 1) { o[i >> 2] = zero16(); PIN(o[i >> 2]); }
        SBAR();
      }
#pragma unroll
      for (int i = 0; i < 4; ++i) fr[i] = fr[16 + i];
      float m0 = max3f(sa[0][0], sa[0][1], sa[1][0]), m1 = max3f(sa[0][2], sa[0][3], sa[1][1]);
      m0 = max3f(m0, sa[1][2], sa[1][3]);
#pragma unroll
      for (int r = 4; r < 16; r += 4) { m0 = max3f(m0, sa[0][r], sa[0][r + 1]); m1 = max3f(m1, sa[0][r + 2], sa[0][r + 3]); m0 = max3f(m0, sa[1][r], sa[1][r + 1]); m1 = max3f(m1, sa[1][r + 2], sa[1][r + 3]); }
      float mx = fmaxf(m0, m1);
      auto rr = __builtin_amdgcn_permlane32_swap(__float_as_uint(mx), __float_as_uint(mx), false, false);
      const float mref = fmaxf(__uint_as_float(rr[0]), __uint_as_float(rr[1]));
#pragma unroll
      for (int i = 0; i < 16; ++i) { sa[0][i] = EXP2(sa[0][i] - mref); sa[1][i] -= mref; negm[i] = -mref; }
      PIN(sa[0]); PIN(sa[1]); PIN(negm);
    }
#define STEP(C, P, LAST)                                                                                                \
    do {                                                                                                                \
      u32x2 pk = {0u, 0u};                                                                                              \
      SBAR();                                                                                                           \
      _Pragma("unroll") for (int i = 0; i < 16; ++i) {                                                                  \
        C[i & 1] = MFMA(fr[i], qf[i >> 1], i < 2 ? negm : C[i & 1]);                                                    \
        if (i == 7) pk = PEEK();                                                                                        \
        if (i == 11) { FREE(it); if (LAST) CHECK1(pk, 0, it + 1); else CHECK2(pk, it + 1);                              \
                       ca = RNEXT(ca); PIN(ca); }                                                                       \
        if (i == 13) { ka0 = RNEXT(ka0); ka1 = ka0 ^ 32; PIN(ka0); PIN(ka1); ++it; }                                    \
        if ((i & 1) == 0) { const int j = i + 4;                                                                        \
          if (j < 16) { fr[j + 1] = KRD(ka0, ka1, SLOT, j + 1); fr[j] = KRD(ka0, ka1, SLOT, j); } else { fr[j + 1] = VRD(ca, 0, j - 15); fr[j] = VRD(ca, 0, j - 16); } } \
        { const int e0 = EA(i), e1 = EA(i + 1);                                                                         \
          _Pragma("unroll") for (int e = e0; e < e1; ++e) P[1][e] = EXP2(P[1][e]);                                      \
          pw[i >> 2][i & 3] = cvtpk(P[i >> 3][(2 * i) & 15], P[i >> 3][((2 * i) & 15) + 1]);                            \
          PIN(pw[i >> 2]); PIN(P[1]); }                                                                                 \
        SBAR();                                                                                                         \
      }                                                                                                                 \
      float m0 = 0.f, m1 = 0.f;                                                                                         \
      _Pragma("unroll") for (int i = 0; i < 16; ++i) {                                                                  \
        o[i & 3] = MFMA(fr[16 + i], __builtin_bit_cast(bf16x8, pw[i >> 2]), o[i & 3]);                                  \
        if (LAST) { if (i == 7) pk = PEEK(); if (i == 11) CHECK1(pk, 1, it); }                                          \
        if ((i & 1) == 0) { const int j = i + 4;                                                                        \
          if (j < 16) { fr[16 + j + 1] = VRD(ca, 0, j + 1); fr[16 + j] = VRD(ca, 0, j); }                               \
          else if (!(LAST)) { fr[16 + j + 1] = KRD(ka0, ka1, SLOT, j - 15); fr[16 + j] = KRD(ka0, ka1, SLOT, j - 16); } \
          else { fr[16 + j + 1] = VRD(ca, SLOT, j - 15); fr[16 + j] = VRD(ca, SLOT, j - 16); } }                        \
        if (i == 1) { m0 = max3f(C[0][0], C[0][1], C[0][2]); m1 = max3f(C[0][3], C[0][4], C[0][5]); m0 = max3f(m0, C[0][6], C[0][7]); m1 = max3f(m1, C[0][8], C[0][9]); \
                      m0 = max3f(m0, C[0][10], C[0][11]); m1 = max3f(m1, C[0][12], C[0][13]); PIN(m0); PIN(m1); }       \
        if (i == 2) { m0 = max3f(m0, C[0][14], C[0][15]); m1 = max3f(m1, C[1][0], C[1][1]); PIN(m0); PIN(m1); }         \
        if (i == 3) { m0 = max3f(m0, C[1][2], C[1][3]); m1 = max3f(m1, C[1][4], C[1][5]); m0 = max3f(m0, C[1][6], C[1][7]); m1 = max3f(m1, C[1][8], C[1][9]); \
                      m0 = max3f(m0, C[1][10], C[1][11]); m1 = max3f(m1, C[1][12], C[1][13]); PIN(m0); PIN(m1); }       \
        if (i == 4) { m0 = max3f(m0, C[1][14], C[1][15]); PIN(m0); }                                                    \
        if (i == 5) {                                                                                                   \
          if (__builtin_expect(__any((m0 > THR) | (m1 > THR)), 0)) {                                                    \
            const float mx = fmaxf(m0, m1);                                                                             \
            auto rr = __builtin_amdgcn_permlane32_swap(__float_as_uint(mx), __float_as_uint(mx), false, false);         \
            const float dl = fmaxf(fmaxf(__uint_as_float(rr[0]), __uint_as_float(rr[1])), 0.f);                         \
            fct = EXP2(-dl); pend = true;                                                                               \
            add_col3(C[0], C[1], negm, lane_id() < 32u ? 1.f : 0.f, -dl);                                               \
          } }                                                                                                           \
        if (i >= 6) { const int e0 = EB(i), e1 = EB(i + 1);                                                             \
          _Pragma("unroll") for (int e = e0; e < e1; ++e) C[0][e] = EXP2(C[0][e]);                                      \
          PIN(C[0]); }                                                                                                  \
        SBAR();                                                                                                         \
        if ((i & 3) == 3) { ls = MFMA16(sel, __builtin_bit_cast(bf16x8, pw[i >> 2]), ls); SBAR(); }                     \
      }                                                                                                                 \
      if (__builtin_expect(pend, 0)) {                                                                                  \
        _Pragma("unroll") for (int d = 0; d < 4; ++d) _Pragma("unroll") for (int e = 0; e < 16; ++e) o[d][e] *= fct;    \
        ls[0] *= fct; pend = false; }                                                                                   \
      _Pragma("unroll") for (int i = 0; i < 4; ++i) fr[i] = fr[32 + i];                                                 \
    } while (0)
#define EA(i) ((i) >= 11 ? 16 : ((i) / 2) * 3 + ((i) & 1))
#define EB(i) ((i) <= 6 ? 0 : ((i) >= 16 ? 16 : (((i) - 6) / 2) * 3 + (((i) - 6) & 1) + ((i) >= 13 ? 1 : 0)))
#pragma unroll 1
    for (int t = 1; t < 7; t += 2) {
      STEP(sb, sa, false);
      STEP(sa, sb, false);
      if (hd == 1 && t == 1) {
        waitvm<0>();
        if (lane_id() == 0) gst(fbase + hp * 768, tag);
      }
    }
    STEP(sb, sa, true);
    { u32x2 pk = {0u, 0u};
#pragma unroll
      for (int i = 0; i < 8; ++i) pw[i >> 2][i & 3] = cvtpk(sb[0][2 * i], sb[0][2 * i + 1]);
      SBAR();
#pragma unroll
      for (int i = 0; i < 16; ++i) {
        o[i & 3] = MFMA(fr[i], __builtin_bit_cast(bf16x8, pw[i >> 2]), o[i & 3]);
        if ((i & 1) == 0 && i + 4 < 16) { fr[i + 5] = VRD(ca, SLOT, i + 5); fr[i + 4] = VRD(ca, SLOT, i + 4); }
        if (i == 11) FREE(it);
        if (i == 13) pk = PEEK();
        if (i < 8) { sb[1][2 * i] = EXP2(sb[1][2 * i]); sb[1][2 * i + 1] = EXP2(sb[1][2 * i + 1]);
                     pw[2 + (i >> 2)][i & 3] = cvtpk(sb[1][2 * i], sb[1][2 * i + 1]); PIN(pw[2 + (i >> 2)]); }
        SBAR();
        if (i == 1 || i == 3 || i == 9 || i == 11) { ls = MFMA16(sel, __builtin_bit_cast(bf16x8, pw[(i >> 3) * 2 + ((i >> 1) & 1)]), ls); SBAR(); }
      }
      const float inv = __builtin_amdgcn_rcpf(ls[0]);
      const unsigned vo = lane_id() * 16 + (unsigned)(2 * gr + hd) * 32768u;
#pragma unroll
      for (int i = 0; i < 8; ++i) {
        of2[i] = pack8(o[i >> 1], i & 1, inv);
        __builtin_amdgcn_raw_buffer_store_b128(__builtin_bit_cast(u32x4, of2[i]), rso, vo + i * 1024, 0, SC1);
      }
      CHECK2(pk, it + 1);
      if (hd == 0) { ka0 = RNEXT(ka0); ka1 = ka0 ^ 32; ca = RNEXT(ca); ++it;
        fr[1] = KRD(ka0, ka1, 0, 1); fr[0] = KRD(ka0, ka1, 0, 0); fr[3] = KRD(ka0, ka1, 0, 3); fr[2] = KRD(ka0, ka1, 0, 2);
        STAMP(3); }
    }
  }
  STAMP(4); STAMPV(17, CYC() - cy0); STAMPV(20, wcyc); STAMPV(21, wcnt); wcyc = 1;
  {
    const unsigned l_ = lane_id(), vo = l_ * 16;
    f32x16 acc[4];
    bf16x8 oa[8], ob[8];
#define OLOAD(dst, hl) do { _Pragma("unroll") for (int i = 0; i < 8; ++i)                                               \
      dst[i] = __builtin_bit_cast(bf16x8, __builtin_amdgcn_raw_buffer_load_b128(rso, vo + (i & 3) * 1024, (unsigned)(hl) * 32768u + (i >> 2) * 4096, SC1)); } while (0)
#define OPAIR(B, FIRST, LAST, MID)                                                                                      \
    do { u32x2 pk = {0u, 0u}; const f32x16 zero = {};                                                                   \
      SBAR();                                                                                                           \
      _Pragma("unroll") for (int i = 0; i < 32; ++i) {                                                                  \
        acc[i & 3] = MFMA(fr[i], B[i >> 2], ((FIRST) && i < 4) ? zero : acc[i & 3]);                                    \
        if (!(LAST)) { if (i == 21) pk = PEEK();                                                                        \
          if (i == 27) { FREE(it); CHECK2(pk, it + 1); ca = RNEXT(ca); PIN(ca); ++it; } }                               \
        if (i == 15) { MID; }                                                                                           \
        if ((i & 1) == 0) { const int j = i + 4;                                                                        \
          if (j < 32) { fr[j + 1] = VRD(ca, 0, j + 1); fr[j] = VRD(ca, 0, j); }                                         \
          else if (!(LAST)) { fr[j + 1] = VRD(ca, 0, j - 31); fr[j] = VRD(ca, 0, j - 32); } }                           \
        SBAR();                                                                                                         \
      }                                                                                                                 \
      if (!(LAST)) { _Pragma("unroll") for (int i = 0; i < 4; ++i) fr[i] = fr[32 + i]; }                                \
    } while (0)
    ca = RNEXT(ca); ++it;
    fr[1] = VRD(ca, 0, 1); fr[0] = VRD(ca, 0, 0); fr[3] = VRD(ca, 0, 3); fr[2] = VRD(ca, 0, 2);
    const char* const peerf = fbase + (4 * gj + (l_ & 3)) * 768;
    OLOAD(oa, 2 * gr);
    OPAIR(of2, true, false, { waitvm<8>(); if (l_ == 0) gst(fbase + hp * 768 + 256, tag); });
    SLOW(gwait(peerf, tag));
    OLOAD(ob, 2 * ((gr + 1) & 3));
    OLOAD(of2, 2 * ((gr + 2) & 3));
    OPAIR(oa, false, false, {});
    OLOAD(oa, 2 * ((gr + 3) & 3));
    OPAIR(ob, false, false, {});
    STAMP(5);
    OPAIR(of2, false, false, {});
    SLOW(gwait(peerf + 256, tag));
    STAMP(6);
    OLOAD(ob, 2 * ((gr + 1) & 3) + 1);
    OLOAD(of2, 2 * ((gr + 2) & 3) + 1);
    OPAIR(oa, false, false, {});
    OLOAD(oa, 2 * ((gr + 3) & 3) + 1);
    OPAIR(ob, false, false, {});
    OPAIR(of2, false, false, {});
    OPAIR(oa, false, true, {});
    STAMP(7);
    const rsrc_t rp = __builtin_amdgcn_make_buffer_rsrc((void*)(WS + WS_P + ((size_t)b * 4 + gr) * 131072 + wid * 2048), 0, 131072 - wid * 2048, 0x00020000);
    f32x16 mine = acc[0];
#pragma unroll
    for (int s = 1; s < 4; ++s) {
      const unsigned va = vo + ((((unsigned)(gj + s) & 3u) * 4u + (unsigned)gj) * 8192u);
      __builtin_amdgcn_raw_buffer_store_b128(pack8n(acc[s], 0), rp, va, 0, SC1);
      __builtin_amdgcn_raw_buffer_store_b128(pack8n(acc[s], 1), rp, va + 1024, 0, SC1);
    }
    waitvm<0>();
    if (l_ == 0) gst(fbase + hp * 768 + 512, tag);
    STAMP(10);
    SLOW(gwait(fbase + (4 * (l_ & 3) + gr) * 768 + 512, tag));
    STAMP(13);
    u32x4 pp[3][2];
#pragma unroll
    for (int s = 0; s < 3; ++s) {
      const unsigned src = (unsigned)(gj + 1 + s) & 3u;
      pp[s][0] = __builtin_amdgcn_raw_buffer_load_b128(rp, vo, ((unsigned)gj * 4 + src) * 8192u, SC1);
      pp[s][1] = __builtin_amdgcn_raw_buffer_load_b128(rp, vo + 1024, ((unsigned)gj * 4 + src) * 8192u, SC1);
    }
    if (wid == 0 && hp == 0 && l_ == 0) gst(WS + WS_E + b * 128, tag);
#pragma unroll
    for (int s = 0; s < 3; ++s)
#pragma unroll
      for (int i = 0; i < 8; ++i) {
        const unsigned w = pp[s][i >> 2][i & 3];
        mine[2 * i] += __uint_as_float(w << 16); mine[2 * i + 1] += __uint_as_float(w & 0xffff0000u);
      }
    u32x4 w0 = pack8n(mine, 0), w1 = pack8n(mine, 1);
    { auto r0 = __builtin_amdgcn_permlane32_swap(w0[0], w0[2], false, false); auto r1 = __builtin_amdgcn_permlane32_swap(w0[1], w0[3], false, false);
      w0 = u32x4{r0[0], r1[0], r0[1], r1[1]};
      auto r2 = __builtin_amdgcn_permlane32_swap(w1[0], w1[2], false, false); auto r3 = __builtin_amdgcn_permlane32_swap(w1[1], w1[3], false, false);
      w1 = u32x4{r2[0], r3[0], r2[1], r3[1]}; }
    char* dst = OUT + ((size_t)(b * 128 + wid * 32 + (l_ & 31)) * 512 + gr * 128 + gj * 32) * 2 + (l_ >> 5) * 16;
    *(u32x4*)dst = w0;
    *(u32x4*)(dst + 32) = w1;
  }
  STAMP(14); STAMPV(23, wcyc); STAMP_DUMP();
}

extern "C" void kernel_launch(void* const* d_in, const int* in_sizes, int n_in, void* d_out, int out_size, void* d_ws, size_t ws_size, hipStream_t stream) {
  const char* x = (const char*)d_in[0]; const char* k = (const char*)d_in[1]; const char* v = (const char*)d_in[2];
  const char* wq = (const char*)d_in[3]; const char* wo = (const char*)d_in[4];
  (void)hipFuncSetAttribute((const void*)mha_fused, hipFuncAttributeMaxDynamicSharedMemorySize, (int)L_END);
  mha_fused<<<256, 384, L_END, stream>>>(x, k, v, wq, wo, (char*)d_ws, (char*)d_out);
}
```

```cpp
#include <hip/hip_runtime.h>
#include <hip/hip_bf16.h>

typedef short    bf16x8 __attribute__((ext_vector_type(8)));
typedef short    s16x4  __attribute__((ext_vector_type(4)));
typedef float    f32x16 __attribute__((ext_vector_type(16)));
typedef float    f32x4  __attribute__((ext_vector_type(4)));
typedef float    f32x2  __attribute__((ext_vector_type(2)));
typedef __bf16   bf16x2 __attribute__((ext_vector_type(2)));
typedef unsigned u32x4  __attribute__((ext_vector_type(4)));
typedef unsigned u32x2  __attribute__((ext_vector_type(2)));
typedef unsigned long long u64;
typedef __amdgpu_buffer_rsrc_t rsrc_t;
#define LAS __attribute__((address_space(3)))
#define GAS __attribute__((address_space(1)))
#define DI __device__ __forceinline__
#define MFMA(a, b, c) __builtin_amdgcn_mfma_f32_32x32x16_bf16((a), (b), (c), 0, 0, 0)
#define MFMA16(a, b, c) __builtin_amdgcn_mfma_f32_16x16x32_bf16((a), (b), (c), 0, 0, 0)

constexpr float QSCALE = 0.08838834764831845f * 1.4426950408889634f;
constexpr float THR = 8.0f;

constexpr unsigned SLOT = 16384, PAIR = 32768, RING = 4 * PAIR;
constexpr unsigned L_PARK = RING, L_END = L_PARK + 32768;
constexpr size_t WS_O = 0;
constexpr size_t WS_P = 16u << 20;
constexpr size_t WS_F = 24u << 20;
constexpr size_t WS_E = 25u << 20;

#define STAMP(i)
#define STAMPV(i, v)
#define STAMP_DECL
#define STAMP_DUMP()
#define CYC() 0ull
#define ASTAMP(i) STAMP(i)

DI unsigned cvtpk(float lo, float hi) { f32x2 v = {lo, hi}; bf16x2 b = __builtin_convertvector(v, bf16x2); return __builtin_bit_cast(unsigned, b); }
DI bf16x8 pack8(const f32x16& x, int s, float sc) {
  u32x4 w = {cvtpk(x[8 * s] * sc, x[8 * s + 1] * sc), cvtpk(x[8 * s + 2] * sc, x[8 * s + 3] * sc), cvtpk(x[8 * s + 4] * sc, x[8 * s + 5] * sc), cvtpk(x[8 * s + 6] * sc, x[8 * s + 7] * sc)};
  return __builtin_bit_cast(bf16x8, w);
}
DI u32x4 pack8n(const f32x16& x, int s) {
  u32x4 w = {cvtpk(x[8 * s], x[8 * s + 1]), cvtpk(x[8 * s + 2], x[8 * s + 3]), cvtpk(x[8 * s + 4], x[8 * s + 5]), cvtpk(x[8 * s + 6], x[8 * s + 7])};
  return w;
}
DI bf16x8 lds_b128(unsigned a) { return *(const LAS bf16x8*)(size_t)a; }
DI s16x4 lds_tr(unsigned a) { return __builtin_amdgcn_ds_read_tr16_b64_v4i16((LAS s16x4*)(size_t)a); }
DI bf16x8 lds_tr2(unsigned a0, unsigned a1) { s16x4 lo = lds_tr(a0), hi = lds_tr(a1); return __builtin_shufflevector(lo, hi, 0, 1, 2, 3, 4, 5, 6, 7); }

template <int N> DI void waitvm() { asm volatile("s_waitcnt vmcnt(%0)" ::"i"(N) : "memory"); }
DI void dma4_nt(rsrc_t r, unsigned v0, unsigned v1, unsigned v2, unsigned v3, unsigned soff, unsigned m0v) {
  asm volatile("s_mov_b32 m0, %0\n\ts_nop 0\n\t"
               "buffer_load_dwordx4 %1, %5, %6 offen nt lds\n\t"
               "buffer_load_dwordx4 %2, %5, %6 offen offset:1024 nt lds\n\t"
               "buffer_load_dwordx4 %3, %5, %6 offen offset:2048 nt lds\n\t"
               "buffer_load_dwordx4 %4, %5, %6 offen offset:3072 nt lds" ::"s"(m0v), "v"(v0), "v"(v1), "v"(v2), "v"(v3), "s"(r), "s"(soff) : "memory");
}
DI void dma4(rsrc_t r, unsigned v0, unsigned v1, unsigned v2, unsigned v3, unsigned soff, unsigned m0v) {
  asm volatile("s_mov_b32 m0, %0\n\ts_nop 0\n\t"
               "buffer_load_dwordx4 %1, %5, %6 offen lds\n\t"
               "buffer_load_dwordx4 %2, %5, %6 offen offset:1024 lds\n\t"
               "buffer_load_dwordx4 %3, %5, %6 offen offset:2048 lds\n\t"
               "buffer_load_dwordx4 %4, %5, %6 offen offset:3072 lds" ::"s"(m0v), "v"(v0), "v"(v1), "v"(v2), "v"(v3), "s"(r), "s"(soff) : "memory");
}
constexpr unsigned GSPIN_MAX = 1u << 15;
#define BAR() asm volatile("s_barrier" ::: "memory")
#define SBAR() __builtin_amdgcn_sched_barrier(0)
#define PIN(x) asm volatile("" : "+v"(x))
DI float max3f(float a, float b, float c) { return __builtin_fmaxf(__builtin_fmaxf(a, b), c); }
DI void add_col3(f32x16& c0, f32x16& c1, f32x16& c2, float aone, float v) {
  asm volatile("s_nop 1\n\tv_mfma_f32_32x32x2_f32 %0, %3, %4, %0\n\tv_mfma_f32_32x32x2_f32 %1, %3, %4, %1\n\tv_mfma_f32_32x32x2_f32 %2, %3, %4, %2\n\ts_nop 15\n\ts_nop 3"
               : "+v"(c0), "+v"(c1), "+v"(c2) : "v"(aone), "v"(v));
}
DI unsigned lane_id() { unsigned l; asm volatile("v_mbcnt_lo_u32_b32 %0, -1, 0\n\tv_mbcnt_hi_u32_b32 %0, -1, %0" : "=v"(l)); return l; }
DI f32x16 zero16() { f32x16 z;
#pragma unroll
  for (int i = 0; i < 16; ++i) { float t; asm volatile("v_mov_b32 %0, 0" : "=v"(t)); z[i] = t; }
  return z; }
#define EXP2(x) __builtin_amdgcn_exp2f(x)
DI u64 gld(const char* p) { return __hip_atomic_load((const GAS u64*)p, __ATOMIC_RELAXED, __HIP_MEMORY_SCOPE_AGENT); }
DI void gst(char* p, u64 v) { __hip_atomic_store((GAS u64*)p, v, __ATOMIC_RELAXED, __HIP_MEMORY_SCOPE_AGENT); }
DI void gwait(const char* p, u64 tag) {
#pragma unroll 1
  for (unsigned s = 0; s < GSPIN_MAX; ++s) { if (__all(gld(p) == tag)) break; __builtin_amdgcn_s_sleep(2); }
  asm volatile("" ::: "memory");
}
constexpr int SC1 = 16;
#define SLOW(X) X
#define GSLOW(n, X) X

__global__ __launch_bounds__(384, 2) void mha_fused(const char* __restrict__ X, const char* __restrict__ K, const char* __restrict__ V,
                                                    const char* __restrict__ WQ, const char* __restrict__ WO, char* WS, char* __restrict__ OUT
) {
  extern __shared__ __attribute__((aligned(16))) char shm[];
  const unsigned lds0 = (unsigned)(size_t)(LAS char*)shm;
  const int tid = threadIdx.x, lane = tid & 63;
  const int wid = __builtin_amdgcn_readfirstlane(tid >> 6);
  const int g = blockIdx.x & 7, idx = blockIdx.x >> 3;
  const int bl = idx & 7, b = 8 * (g & 1) + bl, gj = g >> 1, gr = idx >> 3, hp = 4 * gj + gr;
  STAMP_DECL;
  if (wid >= 4) {
    const unsigned L = (unsigned)wid - 4u;
    __builtin_amdgcn_s_setprio(3);
    const rsrc_t rx = __builtin_amdgcn_make_buffer_rsrc((void*)(X + (size_t)b * 131072 + L * 65536), 0, 131072 - L * 65536, 0x00020000);
    const rsrc_t rw = __builtin_amdgcn_make_buffer_rsrc((void*)(WQ + (size_t)hp * 512 + L * 262144), 0, 4194304 - hp * 512 - L * 262144, 0x00020000);
    const unsigned t4 = (lane >> 4) * 64 + (lane & 3) * 16, r4 = (lane >> 2) & 3;
    unsigned vx[4], vw[4];
#pragma unroll
    for (int j = 0; j < 4; ++j) {
      vx[j] = (lane >> 3) * 1024 + (((lane & 7) ^ ((4 * j + (lane >> 4)) & 7)) * 16) + j * (8192 - 1024);
      vw[j] = r4 * 8192 + t4 + j * (32768 - 1024);
    }
    const unsigned rot = 2u * (unsigned)(gr + (bl >> 1)), sub = (unsigned)bl & 1u;
    const unsigned m0b = lds0 + L * 8192;
#define HALF_X(so, m) do { dma4(rx, vx[0], vx[1], vx[2], vx[3], (so), (m)); dma4(rx, vx[0], vx[1], vx[2], vx[3], (so) + 32768u, (m) + 4096u); } while (0)
#define HALF_W(so, m) do { dma4(rw, vw[0], vw[1], vw[2], vw[3], (so), (m)); dma4(rw, vw[0], vw[1], vw[2], vw[3], (so) + 131072u, (m) + 4096u); } while (0)
#define HALF_O(so, m) do { dma4(ro, vo[0], vo[1], vo[2], vo[3], (so), (m)); dma4(ro, vo[0], vo[1], vo[2], vo[3], (so) + 16384u, (m) + 4096u); } while (0)
#define HALF_K(so, m) do { dma4_nt(rk, vk[0], vk[1], vk[2], vk[3], (so), (m)); dma4_nt(rk, vk[0], vk[1], vk[2], vk[3], (so) + 4096u, (m) + 4096u); } while (0)
#define HALF_V(so, m) do { dma4_nt(rv, vv, vv, vv, vv, (so), (m)); dma4_nt(rv, vv, vv, vv, vv, (so) + 4096u, (m) + 4096u); } while (0)
#define UNIT(u, m) do { const unsigned c_ = (((u) + rot) & 7u) ^ sub; HALF_X(c_ * 128u, (m)); HALF_W(c_ * 524288u, (m) + SLOT); HALF_W(c_ * 524288u + 256u, (m) + 2 * SLOT); } while (0)
#define FLOAD(dst, p) asm volatile("global_load_dwordx2 %0, %1, off sc1" : "=v"(dst) : "v"(p) : "memory")
    UNIT(0u, m0b);
    u64 fv;
    FLOAD(fv, WS + WS_E + b * 128);
    const size_t kvb = ((size_t)b * 16 + hp) * 262144 + L * 8192;
    const rsrc_t rk = __builtin_amdgcn_make_buffer_rsrc((void*)(K + kvb - 896), 0, 262144 + 896 - L * 8192, 0x00020000);
    const rsrc_t rv = __builtin_amdgcn_make_buffer_rsrc((void*)(V + kvb), 0, 262144 - L * 8192, 0x00020000);
    const rsrc_t ro = __builtin_amdgcn_make_buffer_rsrc((void*)(WO + (size_t)gj * 1048576 + gr * 256 + L * 32768), 0, 1048576 - gr * 256 - L * 32768, 0x00020000);
    unsigned vk[4], vo[4];
#pragma unroll
    for (int j = 0; j < 4; ++j) {
      vk[j] = ((lane >> 2) & 7) * 256 + (lane >> 5) * 64 + (((lane & 3) ^ ((2 * (j >> 1) + ((lane >> 4) & 1)) & 3)) * 16) + ((j & 1) ? 0 : 896);
      vo[j] = r4 * 1024 + (((lane >> 4) + gj) & 3) * 64 + (lane & 3) * 16 + j * (4096 - 1024);
    }
    const unsigned vv = r4 * 256 + t4;
    const char* const peerf = WS + WS_F + ((size_t)b * 16 + 4 * gj + (lane & 3)) * 768 + ((lane >> 2) & 3) * 64;
    u32x4 keep = {0u, 0u, 0u, 0u};
    auto issue = [&](unsigned q) {
      const unsigned m = m0b + ((q + 3u) & 3u) * PAIR;
      if (q < 16) {
        const unsigned p = q & 7u, hb = (q & 8u) * SLOT;
        if (p == 0) HALF_K(hb, m); else HALF_V(hb + (p - 1u) * SLOT, m);
        if (p < 7) HALF_K(hb + (p + 1u) * SLOT, m + SLOT); else HALF_V(hb + 7u * SLOT, m + SLOT);
      } else {
        const unsigned s = q - 16u, hl = s == 0 ? 2u * gr + 1u : (s < 5 ? 2u * ((gr + s - 1u) & 3u) : 2u * ((gr + s - 4u) & 3u) + 1u);
        HALF_O(hl * 131072u, m); HALF_O(hl * 131072u + 65536u, m + SLOT);
      }
    };
    waitvm<9>(); BAR();
    UNIT(1u, m0b + 3 * SLOT);
    asm volatile("s_waitcnt vmcnt(24)" : "+v"(fv) :: "memory");
    const u64 tag = (((u64)(unsigned)__builtin_amdgcn_readfirstlane((unsigned)(fv >> 32)) << 32) | (unsigned)__builtin_amdgcn_readfirstlane((unsigned)fv)) + 1ull;
    BAR();
    { unsigned m = m0b + 6 * SLOT;
      UNIT(2u, m);
#pragma unroll 1
      for (unsigned u = 1; u < 6; ++u) {
        m = (m == m0b + 6 * SLOT) ? m0b : m + 3 * SLOT;
        waitvm<24>(); BAR();
        UNIT(u + 2u, m);
      } }
    STAMP(8);
    waitvm<24>(); BAR(); issue(0);
    waitvm<16>(); BAR(); issue(1);
#define WOPF 99
#define LA3END 10
#pragma unroll 1
    for (unsigned j = 0; j < 16; ++j) {
      if (j == 7) { waitvm<40>(); BAR(); }
      if (j == 15) { waitvm<24>(); BAR(); }
      if (j >= 1 && j <= LA3END) waitvm<32>(); else asm volatile("s_waitcnt vmcnt(16)" : "+v"(fv) :: "memory");
      if (j == 13) { if (!__all(fv == tag)) gwait(peerf, tag); }
      BAR();
      if (j == 12) FLOAD(fv, peerf);
      if (j == WOPF) {
#pragma unroll
        for (int i = 0; i < 4; ++i)
          asm volatile("buffer_load_dwordx4 %0, %1, %5, %6 offen\n\tbuffer_load_dwordx4 %0, %2, %5, %6 offen offset:1024\n\t"
                       "buffer_load_dwordx4 %0, %3, %5, %6 offen offset:2048\n\tbuffer_load_dwordx4 %0, %4, %5, %6 offen offset:3072"
                       : "+v"(keep) : "v"(vo[0]), "v"(vo[1]), "v"(vo[2]), "v"(vo[3]), "s"(ro), "s"((unsigned)bl * 131072u + (i >> 1) * 65536u + (i & 1) * 16384u) : "memory");
      }
      if (j == 0) issue(2u);
      if (j < LA3END) issue(j + 3u); else if (j > LA3END) issue(j + 2u);
    }
    issue(18u);
    STAMP(11);
#pragma unroll 1
    for (unsigned s = 0; s < 8; ++s) {
      if (s == 3) { asm volatile("s_waitcnt vmcnt(16)" : "+v"(fv) :: "memory"); if (!__all(fv == tag)) GSLOW(28, gwait(peerf + 256, tag)); }
      else if (s < 6) waitvm<32>(); else if (s == 6) waitvm<16>(); else waitvm<0>();
      BAR();
      if (s == 2) FLOAD(fv, peerf + 256);
      if (s < 5) issue(s + 19u);
    }
    asm volatile("" :: "v"(keep));
    STAMP(1); STAMP_DUMP();
    return;
  }
  STAMP(0);
  const int r32 = lane & 31, hi = lane >> 5;
  const unsigned kbase = lds0 + (r32 >> 3) * 2048 + (r32 & 7) * 64 + ((hi ^ ((r32 >> 2) & 3)) * 16);
  const unsigned vbase = lds0 + hi * 1024 + ((lane >> 2) & 3) * 64 + ((lane >> 4) & 1) * 32 + (lane & 3) * 8;
  u64 tag;
  { const u64 e = gld(WS + WS_E + b * 128) + 1ull;
    tag = ((u64)(unsigned)__builtin_amdgcn_readfirstlane((unsigned)(e >> 32)) << 32) | (unsigned)__builtin_amdgcn_readfirstlane((unsigned)e); }
  const unsigned long long cy0 = CYC(); unsigned long long cyl = cy0; (void)cyl; unsigned wcyc = 1, wcnt = 1; (void)wcyc; (void)wcnt;
#define KRD(a0, a1, OFF, i) lds_b128(((((i) >> 1) & 1) ? (a1) : (a0)) + ((OFF) + 512 * ((i) >> 2) + ((i) & 1) * 8192))
#define VRD(a, OFF, i) lds_tr2((a) + ((OFF) + 4096 * ((i) >> 2) + 256 * ((i) & 3)), (a) + ((OFF) + 4096 * ((i) >> 2) + 256 * ((i) & 3) + 2048))
#define WRD(a, i) lds_tr2((a) + (4096 * ((i) >> 2) + 256 * ((i) & 3)), (a) + (4096 * ((i) >> 2) + 256 * ((i) & 3) + 1024))
#define RNEXT(a) (((a) + PAIR) & (RING - 1))
#define LBAR() do { __builtin_amdgcn_s_waitcnt(0xC07F); BAR(); } while (0)
#define WBAR() LBAR()
  bf16x8 qf[8], fr[36];
  unsigned ca, ka0, ka1;
  f32x16 qa[4];
  {
    const unsigned wbase = lds0 + hi * 2048 + ((lane >> 2) & 3) * 64 + ((lane >> 4) & 1) * 32 + (((lane & 1) << 1) | ((lane >> 1) & 1)) * 8;
    const unsigned xbase = lds0 + (wid * 32 + r32) * 128 + ((hi ^ ((r32 >> 1) & 7)) * 16);
    f32x16 acc[8];
#pragma unroll
    for (int d = 0; d < 8; ++d) { acc[d] = zero16(); PIN(acc[d]); }
    bf16x8 xa_[4], xb_[4], wf[38];
    asm volatile("" : "+s"(tag));
    BAR();
    ASTAMP(9);
    __builtin_amdgcn_s_waitcnt(0xC07F);
    unsigned uo = 0, xa = xbase; ca = wbase + SLOT;
    xa_[0] = lds_b128(xa); wf[1] = WRD(ca, 1); wf[0] = WRD(ca, 0); wf[3] = WRD(ca, 3); wf[2] = WRD(ca, 2);
    xa_[1] = lds_b128(xa ^ 32); wf[5] = WRD(ca, 5); wf[4] = WRD(ca, 4); xa_[2] = lds_b128(xa ^ 64); xa_[3] = lds_b128(xa ^ 96);
#define QUNIT(XC, XN, FIRST, LAST)                                                                                      \
    do {                                                                                                                \
      SBAR();                                                                                                           \
      _Pragma("unroll") for (int i = 0; i < 32; ++i) {                                                                  \
        const int ai = (i >> 4) * 4 + (i & 3);                                                                          \
        acc[ai] = MFMA(wf[i], XC[(i >> 2) & 3], acc[ai]);                                                               \
        if (i == 9) { if (FIRST) BAR(); }                                                                               \
        if (i == 25) { if (!(LAST)) { uo = (uo == 6 * SLOT) ? 0u : uo + 3 * SLOT; xa = xbase + uo; ca = wbase + SLOT + uo; PIN(xa); PIN(ca); } \
                       else { ka0 = kbase + 3 * PAIR; ka1 = ka0 ^ 32; PIN(ka0); PIN(ka1); } }        \
        if (i == ((LAST) ? 28 : 26)) { WBAR(); if (!(LAST)) XN[0] = lds_b128(xa); }                                     \
        if (!(LAST) && i == 27) { XN[1] = lds_b128(xa ^ 32); XN[2] = lds_b128(xa ^ 64); }                               \
        if (!(LAST) && i == 29) XN[3] = lds_b128(xa ^ 96);                                                              \
        if ((i & 1) == 0) { const int j = i + 6;                                                                        \
          if (j < 32 || !(LAST)) { wf[j + 1] = WRD(ca, (j + 1) & 31); wf[j] = WRD(ca, j & 31); }                        \
          else if (j >= 34) { fr[j - 33] = KRD(ka0, ka1, 0, j - 33); fr[j - 34] = KRD(ka0, ka1, 0, j - 34); } }         \
        if ((LAST) && i >= 17 && (i & 1)) { const int d_ = (i - 17) >> 1; qf[d_] = pack8(acc[d_ >> 1], d_ & 1, QSCALE); PIN(qf[d_]); }     \
        SBAR();                                                                                                         \
      }                                                                                                                 \
      if (!(LAST)) { _Pragma("unroll") for (int i = 0; i < 6; ++i) wf[i] = wf[32 + i]; }                                \
    } while (0)
    cyl = CYC();
#pragma unroll 1
    for (int D = 0; D < 3; ++D) { QUNIT(xa_, xb_, D == 0, false); QUNIT(xb_, xa_, false, false); }
    STAMPV(32, CYC() - cyl); STAMPV(33, wcyc); cyl = CYC();
    QUNIT(xa_, xb_, false, false); QUNIT(xb_, xa_, false, true);
    STAMPV(34, CYC() - cyl);
#pragma unroll
    for (int d = 0; d < 4; ++d) qa[d] = acc[4 + d];
    ca = vbase + 3 * PAIR;
  }
  ASTAMP(2); STAMPV(16, CYC() - cy0); STAMPV(18, wcyc); STAMPV(19, wcnt); wcyc = 1; wcnt = 1;
  bf16x8 sel;
  { const unsigned l_ = lane_id(); const short one = (((l_ >> 4) ^ (l_ >> 2)) & 1) ? (short)0 : (short)0x3F80;
#pragma unroll
    for (int i = 0; i < 8; ++i) sel[i] = one; }
  const rsrc_t rso = __builtin_amdgcn_make_buffer_rsrc((void*)(WS + WS_O + ((size_t)b * 32 + gj * 8) * 32768 + wid * 8192), 0, 8 * 32768 - wid * 8192, 0x00020000);
  char* const fbase = WS + WS_F + (size_t)b * 16 * 768 + wid * 64;
  bf16x8 of2[8], ob[8], oc[8];
  f32x16 sa[2], sb[2];
#define OLOAD(dst, hl) do { const unsigned vo_ = lane_id() * 16; _Pragma("unroll") for (int i_ = 0; i_ < 8; ++i_)        \
    dst[i_] = __builtin_bit_cast(bf16x8, __builtin_amdgcn_raw_buffer_load_b128(rso, vo_ + (i_ & 3) * 1024, (unsigned)(hl) * 32768u + (i_ >> 2) * 4096, SC1)); } while (0)
#define TILE0(PARK)                                                                                                     \
  do { const f32x16 zero = {}; const unsigned pa = lds0 + L_PARK + wid * 8192 + lane_id() * 16;                         \
    SBAR();                                                                                                             \
    _Pragma("unroll") for (int i = 0; i < 16; ++i) {                                                                    \
      sa[i & 1] = MFMA(fr[i], qf[i >> 1], i < 2 ? zero : sa[i & 1]);                                                    \
      if ((i & 1) == 0) { const int j = i + 4;                                                                          \
        if (j < 16) { fr[j + 1] = KRD(ka0, ka1, 0, j + 1); fr[j] = KRD(ka0, ka1, 0, j); } else { fr[j + 1] = KRD(ka0, ka1, SLOT, j - 15); fr[j] = KRD(ka0, ka1, SLOT, j - 16); } } \
      if ((PARK) && (i & 1)) { const int d_ = i >> 1; const bf16x8 w_ = pack8(qa[d_ >> 1], d_ & 1, QSCALE);             \
        *(LAS bf16x8*)(size_t)(pa + d_ * 1024) = w_; }                                                                  \
      SBAR();                                                                                                           \
    }                                                                                                                   \
    _Pragma("unroll") for (int i = 0; i < 4; ++i) fr[i] = fr[16 + i];                                                   \
  } while (0)
#define OFINISH(HD)                                                                                                     \
  do { const float inv = __builtin_amdgcn_rcpf(ls[0]);                                                                  \
    const unsigned vo_ = lane_id() * 16 + (unsigned)(2 * gr + (HD)) * 32768u;                                           \
    _Pragma("unroll") for (int i_ = 0; i_ < 8; ++i_) {                                                                  \
      of2[i_] = pack8(o[i_ >> 1], i_ & 1, inv);                                                                         \
      __builtin_amdgcn_raw_buffer_store_b128(__builtin_bit_cast(u32x4, of2[i_]), rso, vo_ + i_ * 1024, 0, SC1); }       \
  } while (0)
  f32x16 o[4];
  f32x4 ls;
  TILE0(true);
#pragma unroll 1
  for (int hd = 0;; ++hd) {
    f32x16 negm;
    ls = f32x4{0.f, 0.f, 0.f, 0.f};
    u32x4 pw[4];
    float fct = 1.f;
    bool pend = false;
    { const f32x16 zero = {};
      u32x4 z4 = {0u, 0u, 0u, 0u}; PIN(z4);
#pragma unroll
      for (int d = 0; d < 4; ++d) { o[d] = MFMA(__builtin_bit_cast(bf16x8, z4), __builtin_bit_cast(bf16x8, z4), zero); PIN(z4); }
      float m0 = max3f(sa[0][0], sa[0][1], sa[1][0]), m1 = max3f(sa[0][2], sa[0][3], sa[1][1]);
      m0 = max3f(m0, sa[1][2], sa[1][3]);
#pragma unroll
      for (int r = 4; r < 16; r += 4) { m0 = max3f(m0, sa[0][r], sa[0][r + 1]); m1 = max3f(m1, sa[0][r + 2], sa[0][r + 3]); m0 = max3f(m0, sa[1][r], sa[1][r + 1]); m1 = max3f(m1, sa[1][r + 2], sa[1][r + 3]); }
      float mx = fmaxf(m0, m1);
      auto rr = __builtin_amdgcn_permlane32_swap(__float_as_uint(mx), __float_as_uint(mx), false, false);
      const float mref = fmaxf(__uint_as_float(rr[0]), __uint_as_float(rr[1]));
#pragma unroll
      for (int i = 0; i < 16; ++i) { sa[0][i] = EXP2(sa[0][i] - mref); sa[1][i] -= mref; negm[i] = -mref; }
      PIN(sa[0]); PIN(sa[1]); PIN(negm);
    }
#define STEP(C, P, LAST)                                                                                                \
    do {                                                                                                                \
      SBAR();                                                                                                           \
      _Pragma("unroll") for (int i = 0; i < 16; ++i) {                                                                  \
        C[i & 1] = MFMA(fr[i], qf[i >> 1], i < 2 ? negm : C[i & 1]);                                                    \
        if (i == 11) { ca = RNEXT(ca); PIN(ca); }                                                                       \
        if (i == 12) WBAR();                                                                                            \
        if (i == 13) { ka0 = RNEXT(ka0); ka1 = ka0 ^ 32; PIN(ka0); PIN(ka1); }                                          \
        if ((i & 1) == 0) { const int j = i + 4;                                                                        \
          if (j < 16) { fr[j + 1] = KRD(ka0, ka1, SLOT, j + 1); fr[j] = KRD(ka0, ka1, SLOT, j); } else { fr[j + 1] = VRD(ca, 0, j - 15); fr[j] = VRD(ca, 0, j - 16); } } \
        { const int e0 = EA(i), e1 = EA(i + 1);                                                                         \
          _Pragma("unroll") for (int e = e0; e < e1; ++e) P[1][e] = EXP2(P[1][e]);                                      \
          pw[i >> 2][i & 3] = cvtpk(P[i >> 3][(2 * i) & 15], P[i >> 3][((2 * i) & 15) + 1]);                            \
          PIN(pw[i >> 2]); PIN(P[1]); }                                                                                 \
        SBAR();                                                                                                         \
      }                                                                                                                 \
      float m0 = 0.f, m1 = 0.f;                                                                                         \
      _Pragma("unroll") for (int i = 0; i < 16; ++i) {                                                                  \
        o[i & 3] = MFMA(fr[16 + i], __builtin_bit_cast(bf16x8, pw[i >> 2]), o[i & 3]);                                  \
        if ((LAST) && i == 12) WBAR();                                                                                  \
        if ((i & 1) == 0) { const int j = i + 4;                                                                        \
          if (j < 16) { fr[16 + j + 1] = VRD(ca, 0, j + 1); fr[16 + j] = VRD(ca, 0, j); }                               \
          else if (!(LAST)) { fr[16 + j + 1] = KRD(ka0, ka1, SLOT, j - 15); fr[16 + j] = KRD(ka0, ka1, SLOT, j - 16); } \
          else { fr[16 + j + 1] = VRD(ca, SLOT, j - 15); fr[16 + j] = VRD(ca, SLOT, j - 16); } }                        \
        if (i == 1) { m0 = max3f(C[0][0], C[0][1], C[0][2]); m1 = max3f(C[0][3], C[0][4], C[0][5]); m0 = max3f(m0, C[0][6], C[0][7]); m1 = max3f(m1, C[0][8], C[0][9]); \
                      m0 = max3f(m0, C[0][10], C[0][11]); m1 = max3f(m1, C[0][12], C[0][13]); PIN(m0); PIN(m1); }       \
        if (i == 2) { m0 = max3f(m0, C[0][14], C[0][15]); m1 = max3f(m1, C[1][0], C[1][1]); PIN(m0); PIN(m1); }         \
        if (i == 3) { m0 = max3f(m0, C[1][2], C[1][3]); m1 = max3f(m1, C[1][4], C[1][5]); m0 = max3f(m0, C[1][6], C[1][7]); m1 = max3f(m1, C[1][8], C[1][9]); \
                      m0 = max3f(m0, C[1][10], C[1][11]); m1 = max3f(m1, C[1][12], C[1][13]); PIN(m0); PIN(m1); }       \
        if (i == 4) { m0 = max3f(m0, C[1][14], C[1][15]); PIN(m0); }                                                    \
        if (i == 5) {                                                                                                   \
          if (__builtin_expect(__any((m0 > THR) | (m1 > THR)), 0)) {                                                    \
            const float mx = fmaxf(m0, m1);                                                                             \
            auto rr = __builtin_amdgcn_permlane32_swap(__float_as_uint(mx), __float_as_uint(mx), false, false);         \
            const float dl = fmaxf(fmaxf(__uint_as_float(rr[0]), __uint_as_float(rr[1])), 0.f);                         \
            fct = EXP2(-dl); pend = true;                                                                               \
            add_col3(C[0], C[1], negm, lane_id() < 32u ? 1.f : 0.f, -dl);                                               \
          } }                                                                                                           \
        if (i >= 6) { const int e0 = EB(i), e1 = EB(i + 1);                                                             \
          _Pragma("unroll") for (int e = e0; e < e1; ++e) C[0][e] = EXP2(C[0][e]);                                      \
          PIN(C[0]); }                                                                                                  \
        SBAR();                                                                                                         \
        if ((i & 3) == 3) { ls = MFMA16(sel, __builtin_bit_cast(bf16x8, pw[i >> 2]), ls); SBAR(); }                     \
      }                                                                                                                 \
      if (__builtin_expect(pend, 0)) {                                                                                  \
        _Pragma("unroll") for (int d = 0; d < 4; ++d) _Pragma("unroll") for (int e = 0; e < 16; ++e) o[d][e] *= fct;    \
        ls[0] *= fct; pend = false; }                                                                                   \
      _Pragma("unroll") for (int i = 0; i < 4; ++i) fr[i] = fr[32 + i];                                                 \
    } while (0)
#define EA(i) ((i) >= 11 ? 16 : ((i) / 2) * 3 + ((i) & 1))
#define EB(i) ((i) <= 6 ? 0 : ((i) >= 16 ? 16 : (((i) - 6) / 2) * 3 + (((i) - 6) & 1) + ((i) >= 13 ? 1 : 0)))
#pragma unroll 1
    for (int t = 1; t < 7; t += 2) {
      STEP(sb, sa, false);
      STEP(sa, sb, false);
      if (hd == 1 && t == 1) {
        waitvm<0>();
        if (lane_id() == 0) gst(fbase + hp * 768, tag);
      }
    }
    STEP(sb, sa, true);
    {
#pragma unroll
      for (int i = 0; i < 8; ++i) pw[i >> 2][i & 3] = cvtpk(sb[0][2 * i], sb[0][2 * i + 1]);
      SBAR();
#pragma unroll
      for (int i = 0; i < 16; ++i) {
        o[i & 3] = MFMA(fr[i], __builtin_bit_cast(bf16x8, pw[i >> 2]), o[i & 3]);
        if ((i & 1) == 0 && i + 4 < 16) { fr[i + 5] = VRD(ca, SLOT, i + 5); fr[i + 4] = VRD(ca, SLOT, i + 4); }
        if (i < 8) { sb[1][2 * i] = EXP2(sb[1][2 * i]); sb[1][2 * i + 1] = EXP2(sb[1][2 * i + 1]);
                     pw[2 + (i >> 2)][i & 3] = cvtpk(sb[1][2 * i], sb[1][2 * i + 1]); PIN(pw[2 + (i >> 2)]); }
        SBAR();
        if (i == 1 || i == 3 || i == 9 || i == 11) { ls = MFMA16(sel, __builtin_bit_cast(bf16x8, pw[(i >> 3) * 2 + ((i >> 1) & 1)]), ls); SBAR(); }
      }
      if (hd == 1) break;
      OFINISH(0);
      {
        const unsigned pa = lds0 + L_PARK + wid * 8192 + lane_id() * 16;
#pragma unroll
        for (int i = 0; i < 8; ++i) qf[i] = lds_b128(pa + i * 1024);
#pragma unroll
        for (int i = 0; i < 8; ++i) *(LAS bf16x8*)(size_t)(pa + i * 1024) = of2[i];
        WBAR();
        ka0 = RNEXT(ka0); ka1 = ka0 ^ 32; ca = RNEXT(ca);
        fr[1] = KRD(ka0, ka1, 0, 1); fr[0] = KRD(ka0, ka1, 0, 0); fr[3] = KRD(ka0, ka1, 0, 3); fr[2] = KRD(ka0, ka1, 0, 2);
        ASTAMP(3);
        TILE0(false);
      }
    }
  }
  OLOAD(ob, 2 * ((gr + 1) & 3)); OLOAD(oc, 2 * ((gr + 2) & 3));
  OFINISH(1);
  ASTAMP(4); STAMPV(17, CYC() - cy0); STAMPV(20, wcyc); STAMPV(21, wcnt); wcyc = 1; cyl = CYC();
  {
    const unsigned l_ = lane_id(), vo = l_ * 16;
    f32x16 acc[4];
    bf16x8 oa[8], wf[38];
#define FIDX(NC, i) ((NC) == 4 ? (i) : ((i) / 3) * 4 + 1 + (i) % 3)
#define OLD1(dst, hl, i_) dst[i_] = __builtin_bit_cast(bf16x8, __builtin_amdgcn_raw_buffer_load_b128(rso, vo + ((i_) & 3) * 1024, (unsigned)(hl) * 32768u + ((i_) >> 2) * 4096, SC1))
#define OPAIR(B, FIRST, NC, NN, MID)                                                                                    \
    do { const f32x16 zero = {};                                                                                        \
      SBAR();                                                                                                           \
      _Pragma("unroll") for (int i = 0; i < 8 * (NC); ++i) {                                                            \
        const int f_ = FIDX(NC, i);                                                                                     \
        acc[f_ & 3] = MFMA(wf[i], B[f_ >> 2], ((FIRST) && i < 4) ? zero : acc[f_ & 3]);                                 \
        if ((NN) && i == 8 * (NC) - 6) { WBAR(); ca = RNEXT(ca); PIN(ca); }                                             \
        { MID; }                                                                                                        \
        if ((i & 1) == 0) { const int j = i + 6;                                                                        \
          if (j < 8 * (NC)) { wf[j + 1] = VRD(ca, 0, FIDX(NC, j + 1)); wf[j] = VRD(ca, 0, FIDX(NC, j)); }               \
          else if (NN) { wf[j + 1] = VRD(ca, 0, FIDX(NN, j + 1 - 8 * (NC))); wf[j] = VRD(ca, 0, FIDX(NN, j - 8 * (NC))); } } \
        SBAR();                                                                                                         \
      }                                                                                                                 \
      if (NN) { _Pragma("unroll") for (int i = 0; i < 6; ++i) wf[i] = wf[8 * (NC) + i]; }                               \
    } while (0)
    WBAR();
    ca = RNEXT(ca);
#pragma unroll
    for (int i = 0; i < 6; i += 2) { wf[i + 1] = VRD(ca, 0, i + 1); wf[i] = VRD(ca, 0, i); }
    OPAIR(of2, true, 4, 4, { if (i == 19) { GSLOW(30, waitvm<0>()); if (l_ == 0) gst(fbase + hp * 768 + 256, tag); }
                             if ((i & 1) && i < 16) oa[i >> 1] = lds_b128(lds0 + L_PARK + wid * 8192 + vo + (i >> 1) * 1024); });
    OPAIR(oa, false, 4, 4, { if (i >= 12 && i < 20) OLD1(of2, 2 * ((gr + 3) & 3), (i - 12) & 7); });
    OPAIR(ob, false, 4, 4, {});
    ASTAMP(5);
    OPAIR(oc, false, 4, 3, { if (i >= 10 && i < 18) OLD1(oa, 2 * ((gr + 1) & 3) + 1, (i - 10) & 7); if (i >= 18 && i < 26) OLD1(ob, 2 * ((gr + 2) & 3) + 1, (i - 18) & 7); });
    ASTAMP(6);
    OPAIR(of2, false, 3, 3, { if (i >= 9 && i < 17) OLD1(oc, 2 * ((gr + 3) & 3) + 1, (i - 9) & 7); });
    OPAIR(oa, false, 3, 3, {});
    OPAIR(ob, false, 3, 3, {});
    OPAIR(oc, false, 3, 0, {});
    ASTAMP(7); STAMPV(25, CYC() - cyl);
    const rsrc_t rp = __builtin_amdgcn_make_buffer_rsrc((void*)(WS + WS_P + ((size_t)b * 4 + gr) * 131072 + wid * 2048), 0, 131072 - wid * 2048, 0x00020000);
    { unsigned c4[4];
      c4[3] = ca; c4[0] = RNEXT(ca); c4[1] = RNEXT(c4[0]); c4[2] = RNEXT(c4[1]);
#pragma unroll
      for (int i = 0; i < 6; i += 2) { wf[i + 1] = VRD(c4[0], 0, 4 * (i + 1)); wf[i] = VRD(c4[0], 0, 4 * i); }
      SBAR();
#pragma unroll
      for (int i = 0; i < 32; ++i) {
        const bf16x8 bo = (i >> 3) == 0 ? of2[i & 7] : (i >> 3) == 1 ? oa[i & 7] : (i >> 3) == 2 ? ob[i & 7] : oc[i & 7];
        acc[0] = MFMA(wf[i], bo, acc[0]);
        if ((i & 1) == 0 && i + 6 < 32) { const int j = i + 6; wf[j + 1] = VRD(c4[(j + 1) >> 3], 0, 4 * ((j + 1) & 7)); wf[j] = VRD(c4[j >> 3], 0, 4 * (j & 7)); }
        if (i < 6) { const int s = 1 + (i >> 1);
          const unsigned va = vo + ((((unsigned)(gj + s) & 3u) * 4u + (unsigned)gj) * 8192u);
          __builtin_amdgcn_raw_buffer_store_b128(pack8n(acc[s], i & 1), rp, va + (i & 1) * 1024, 0, SC1); }
        if (i == 27) { GSLOW(31, waitvm<0>()); if (l_ == 0) gst(fbase + hp * 768 + 512, tag); }
        SBAR();
      } }
    f32x16 mine = acc[0];
    ASTAMP(10);
    GSLOW(29, gwait(fbase + (4 * (l_ & 3) + gr) * 768 + 512, tag));
    ASTAMP(13);
    u32x4 pp[3][2];
#pragma unroll
    for (int s = 0; s < 3; ++s) {
      const unsigned src = (unsigned)(gj + 1 + s) & 3u;
      pp[s][0] = __builtin_amdgcn_raw_buffer_load_b128(rp, vo, ((unsigned)gj * 4 + src) * 8192u, SC1);
      pp[s][1] = __builtin_amdgcn_raw_buffer_load_b128(rp, vo + 1024, ((unsigned)gj * 4 + src) * 8192u, SC1);
    }
    if (wid == 0 && hp == 0 && l_ == 0) gst(WS + WS_E + b * 128, tag);
#pragma unroll
    for (int s = 0; s < 3; ++s)
#pragma unroll
      for (int i = 0; i < 8; ++i) {
        const unsigned w = pp[s][i >> 2][i & 3];
        mine[2 * i] += __uint_as_float(w << 16); mine[2 * i + 1] += __uint_as_float(w & 0xffff0000u);
      }
    u32x4 w0 = pack8n(mine, 0), w1 = pack8n(mine, 1);
    { auto r0 = __builtin_amdgcn_permlane32_swap(w0[0], w0[2], false, false); auto r1 = __builtin_amdgcn_permlane32_swap(w0[1], w0[3], false, false);
      w0 = u32x4{r0[0], r1[0], r0[1], r1[1]};
      auto r2 = __builtin_amdgcn_permlane32_swap(w1[0], w1[2], false, false); auto r3 = __builtin_amdgcn_permlane32_swap(w1[1], w1[3], false, false);
      w1 = u32x4{r2[0], r3[0], r2[1], r3[1]}; }
    char* dst = OUT + ((size_t)(b * 128 + wid * 32 + (l_ & 31)) * 512 + gr * 128 + gj * 32) * 2 + (l_ >> 5) * 16;
    *(u32x4*)dst = w0;
    *(u32x4*)(dst + 32) = w1;
  }
  STAMPV(59, CYC() - cy0); STAMP(14); STAMPV(23, wcyc); STAMPV(26, 1 + (__builtin_amdgcn_s_getreg((20 << 0) | (0 << 6) | (3 << 11)) & 15)); STAMP_DUMP();
}

extern "C" void kernel_launch(void* const* d_in, const int* in_sizes, int n_in, void* d_out, int out_size, void* d_ws, size_t ws_size, hipStream_t stream) {
  const char* x = (const char*)d_in[0]; const char* k = (const char*)d_in[1]; const char* v = (const char*)d_in[2];
  const char* wq = (const char*)d_in[3]; const char* wo = (const char*)d_in[4];
  (void)hipFuncSetAttribute((const void*)mha_fused, hipFuncAttributeMaxDynamicSharedMemorySize, (int)L_END);
  mha_fused<<<256, 384, L_END, stream>>>(x, k, v, wq, wo, (char*)d_ws, (char*)d_out);
}
```

```cpp
#define WARM_TLB
#define WARM_CODE 28672
#include <hip/hip_runtime.h>
#include <hip/hip_bf16.h>

typedef short    bf16x8 __attribute__((ext_vector_type(8)));
typedef short    s16x4  __attribute__((ext_vector_type(4)));
typedef float    f32x16 __attribute__((ext_vector_type(16)));
typedef float    f32x4  __attribute__((ext_vector_type(4)));
typedef float    f32x2  __attribute__((ext_vector_type(2)));
typedef __bf16   bf16x2 __attribute__((ext_vector_type(2)));
typedef unsigned u32x4  __attribute__((ext_vector_type(4)));
typedef unsigned u32x2  __attribute__((ext_vector_type(2)));
typedef unsigned long long u64;
typedef __amdgpu_buffer_rsrc_t rsrc_t;
#define LAS __attribute__((address_space(3)))
#define GAS __attribute__((address_space(1)))
#define DI __device__ __forceinline__
#define MFMA(a, b, c) __builtin_amdgcn_mfma_f32_32x32x16_bf16((a), (b), (c), 0, 0, 0)
#define MFMA16(a, b, c) __builtin_amdgcn_mfma_f32_16x16x32_bf16((a), (b), (c), 0, 0, 0)

constexpr float QSCALE = 0.08838834764831845f * 1.4426950408889634f;
constexpr float THR = 8.0f;

constexpr unsigned SLOT = 16384, PAIR = 32768, RING = 4 * PAIR;
constexpr unsigned L_PARK = RING, L_END = L_PARK + 32768;
constexpr size_t WS_B = 2u << 20;
constexpr size_t WS_O = 4096;
constexpr size_t WS_P = WS_O + (1u << 20);
constexpr size_t WS_F = WS_P + (512u << 10);
constexpr size_t WS_E = WS_F + 16384;
constexpr size_t WS_T = WS_E + 4096;

#define STAMP(i)
#define STAMPV(i, v)
#define STAMP_DECL
#define STAMP_DUMP()
#define CYC() 0ull
#define ASTAMP(i) STAMP(i)

DI unsigned cvtpk(float lo, float hi) { f32x2 v = {lo, hi}; bf16x2 b = __builtin_convertvector(v, bf16x2); return __builtin_bit_cast(unsigned, b); }
DI bf16x8 pack8(const f32x16& x, int s, float sc) {
  u32x4 w = {cvtpk(x[8 * s] * sc, x[8 * s + 1] * sc), cvtpk(x[8 * s + 2] * sc, x[8 * s + 3] * sc), cvtpk(x[8 * s + 4] * sc, x[8 * s + 5] * sc), cvtpk(x[8 * s + 6] * sc, x[8 * s + 7] * sc)};
  return __builtin_bit_cast(bf16x8, w);
}
DI u32x4 pack8n(const f32x16& x, int s) {
  u32x4 w = {cvtpk(x[8 * s], x[8 * s + 1]), cvtpk(x[8 * s + 2], x[8 * s + 3]), cvtpk(x[8 * s + 4], x[8 * s + 5]), cvtpk(x[8 * s + 6], x[8 * s + 7])};
  return w;
}
DI bf16x8 lds_b128(unsigned a) { return *(const LAS bf16x8*)(size_t)a; }
DI s16x4 lds_tr(unsigned a) { return __builtin_amdgcn_ds_read_tr16_b64_v4i16((LAS s16x4*)(size_t)a); }
DI bf16x8 lds_tr2(unsigned a0, unsigned a1) { s16x4 lo = lds_tr(a0), hi = lds_tr(a1); return __builtin_shufflevector(lo, hi, 0, 1, 2, 3, 4, 5, 6, 7); }

template <int N> DI void waitvm() { asm volatile("s_waitcnt vmcnt(%0)" ::"i"(N) : "memory"); }
DI void dma4_nt(rsrc_t r, unsigned v0, unsigned v1, unsigned v2, unsigned v3, unsigned soff, unsigned m0v) {
  asm volatile("s_mov_b32 m0, %0\n\ts_nop 0\n\t"
               "buffer_load_dwordx4 %1, %5, %6 offen nt lds\n\t"
               "buffer_load_dwordx4 %2, %5, %6 offen offset:1024 nt lds\n\t"
               "buffer_load_dwordx4 %3, %5, %6 offen offset:2048 nt lds\n\t"
               "buffer_load_dwordx4 %4, %5, %6 offen offset:3072 nt lds" ::"s"(m0v), "v"(v0), "v"(v1), "v"(v2), "v"(v3), "s"(r), "s"(soff) : "memory");
}
DI void dma4(rsrc_t r, unsigned v0, unsigned v1, unsigned v2, unsigned v3, unsigned soff, unsigned m0v) {
  asm volatile("s_mov_b32 m0, %0\n\ts_nop 0\n\t"
               "buffer_load_dwordx4 %1, %5, %6 offen lds\n\t"
               "buffer_load_dwordx4 %2, %5, %6 offen offset:1024 lds\n\t"
               "buffer_load_dwordx4 %3, %5, %6 offen offset:2048 lds\n\t"
               "buffer_load_dwordx4 %4, %5, %6 offen offset:3072 lds" ::"s"(m0v), "v"(v0), "v"(v1), "v"(v2), "v"(v3), "s"(r), "s"(soff) : "memory");
}
constexpr unsigned GSPIN_MAX = 1u << 15;
#define BAR() asm volatile("s_barrier" ::: "memory")
#define SBAR() __builtin_amdgcn_sched_barrier(0)
#define PIN(x) asm volatile("" : "+v"(x))
DI float max3f(float a, float b, float c) { return __builtin_fmaxf(__builtin_fmaxf(a, b), c); }
DI void add_col3(f32x16& c0, f32x16& c1, f32x16& c2, float aone, float v) {
  asm volatile("s_nop 1\n\tv_mfma_f32_32x32x2_f32 %0, %3, %4, %0\n\tv_mfma_f32_32x32x2_f32 %1, %3, %4, %1\n\tv_mfma_f32_32x32x2_f32 %2, %3, %4, %2\n\ts_nop 15\n\ts_nop 3"
               : "+v"(c0), "+v"(c1), "+v"(c2) : "v"(aone), "v"(v));
}
DI unsigned lane_id() { unsigned l; asm volatile("v_mbcnt_lo_u32_b32 %0, -1, 0\n\tv_mbcnt_hi_u32_b32 %0, -1, %0" : "=v"(l)); return l; }
DI f32x16 zero16() { f32x16 z;
#pragma unroll
  for (int i = 0; i < 16; ++i) { float t; asm volatile("v_mov_b32 %0, 0" : "=v"(t)); z[i] = t; }
  return z; }
#define EXP2(x) __builtin_amdgcn_exp2f(x)
DI u64 gld(const char* p) { return __hip_atomic_load((const GAS u64*)p, __ATOMIC_RELAXED, __HIP_MEMORY_SCOPE_AGENT); }
DI void gst(char* p, u64 v) { __hip_atomic_store((GAS u64*)p, v, __ATOMIC_RELAXED, __HIP_MEMORY_SCOPE_AGENT); }
DI void gwait(const char* p, u64 tag) {
#pragma unroll 1
  for (unsigned s = 0; s < GSPIN_MAX; ++s) { if (__all(gld(p) == tag)) break; __builtin_amdgcn_s_sleep(2); }
  asm volatile("" ::: "memory");
}
constexpr int SC1 = 16;
#define SLOW(X) X
#define GSLOW(n, X) X

__global__ __launch_bounds__(384, 2) void mha_fused(const char* __restrict__ X, const char* __restrict__ K, const char* __restrict__ V,
                                                    const char* __restrict__ WQ, const char* __restrict__ WO, char* WS, char* __restrict__ OUT
) {
  extern __shared__ __attribute__((aligned(16))) char shm[];
  const unsigned lds0 = (unsigned)(size_t)(LAS char*)shm;
  const int tid = threadIdx.x, lane = tid & 63;
  const int wid = __builtin_amdgcn_readfirstlane(tid >> 6);
  const int g = blockIdx.x & 7, idx = blockIdx.x >> 3;
  const int bl = idx & 7, b = 8 * (g & 1) + bl, gj = g >> 1, gr = idx >> 3, hp = 4 * gj + gr;
  char* const WB = WS + (size_t)b * WS_B;
  u64 pc0; asm volatile("s_getpc_b64 %0" : "=s"(pc0));
  STAMP_DECL;
  if (wid >= 4) {
    const unsigned L = (unsigned)wid - 4u;
    __builtin_amdgcn_s_setprio(3);
    const rsrc_t rx = __builtin_amdgcn_make_buffer_rsrc((void*)(X + (size_t)b * 131072 + L * 65536), 0, 131072 - L * 65536, 0x00020000);
    const rsrc_t rw = __builtin_amdgcn_make_buffer_rsrc((void*)(WQ + (size_t)hp * 512 + L * 262144), 0, 4194304 - hp * 512 - L * 262144, 0x00020000);
    const unsigned t4 = (lane >> 4) * 64 + (lane & 3) * 16, r4 = (lane >> 2) & 3;
    unsigned vx[4], vw[4];
#pragma unroll
    for (int j = 0; j < 4; ++j) {
      vx[j] = (lane >> 3) * 1024 + (((lane & 7) ^ ((4 * j + (lane >> 4)) & 7)) * 16) + j * (8192 - 1024);
      vw[j] = r4 * 8192 + t4 + j * (32768 - 1024);
    }
    const unsigned rot = 2u * (unsigned)(gr + (bl >> 1)), sub = (unsigned)bl & 1u;
    const unsigned m0b = lds0 + L * 8192;
#define HALF_X(so, m) do { dma4(rx, vx[0], vx[1], vx[2], vx[3], (so), (m)); dma4(rx, vx[0], vx[1], vx[2], vx[3], (so) + 32768u, (m) + 4096u); } while (0)
#define HALF_W(so, m) do { dma4(rw, vw[0], vw[1], vw[2], vw[3], (so), (m)); dma4(rw, vw[0], vw[1], vw[2], vw[3], (so) + 131072u, (m) + 4096u); } while (0)
#define HALF_O(so, m) do { dma4(ro, vo[0], vo[1], vo[2], vo[3], (so), (m)); dma4(ro, vo[0], vo[1], vo[2], vo[3], (so) + 16384u, (m) + 4096u); } while (0)
#define PACE 63
#define HALF_K(so, m) do { waitvm<PACE>(); dma4_nt(rk, vk[0], vk[1], vk[2], vk[3], (so), (m)); waitvm<PACE>(); dma4_nt(rk, vk[0], vk[1], vk[2], vk[3], (so) + 4096u, (m) + 4096u); } while (0)
#define HALF_V(so, m) do { waitvm<PACE>(); dma4_nt(rv, vv, vv, vv, vv, (so), (m)); waitvm<PACE>(); dma4_nt(rv, vv, vv, vv, vv, (so) + 4096u, (m) + 4096u); } while (0)
#define UNIT(u, m) do { const unsigned c_ = (((u) + rot) & 7u) ^ sub; HALF_X(c_ * 128u, (m)); HALF_W(c_ * 524288u, (m) + SLOT); HALF_W(c_ * 524288u + 256u, (m) + 2 * SLOT); } while (0)
#define FLOAD(dst, p) asm volatile("global_load_dwordx2 %0, %1, off sc1" : "=v"(dst) : "v"(p) : "memory")
    UNIT(0u, m0b);
    u64 fv;
    FLOAD(fv, WB + WS_E);
    const size_t kvb = ((size_t)b * 16 + hp) * 262144 + L * 8192;
    const rsrc_t rk = __builtin_amdgcn_make_buffer_rsrc((void*)(K + kvb - 896), 0, 262144 + 896 - L * 8192, 0x00020000);
    const rsrc_t rv = __builtin_amdgcn_make_buffer_rsrc((void*)(V + kvb), 0, 262144 - L * 8192, 0x00020000);
    const rsrc_t ro = __builtin_amdgcn_make_buffer_rsrc((void*)(WO + (size_t)gj * 1048576 + gr * 256 + L * 32768), 0, 1048576 - gr * 256 - L * 32768, 0x00020000);
    unsigned vk[4], vo[4];
#pragma unroll
    for (int j = 0; j < 4; ++j) {
      vk[j] = ((lane >> 2) & 7) * 256 + (lane >> 5) * 64 + (((lane & 3) ^ ((2 * (j >> 1) + ((lane >> 4) & 1)) & 3)) * 16) + ((j & 1) ? 0 : 896);
      vo[j] = r4 * 1024 + (((lane >> 4) + gj) & 3) * 64 + (lane & 3) * 16 + j * (4096 - 1024);
    }
    const unsigned vv = r4 * 256 + t4;
    const char* const peerf = WB + WS_F + (4 * gj + (lane & 3)) * 768 + ((lane >> 2) & 3) * 64;
    u32x4 keep = {0u, 0u, 0u, 0u};
    auto issue = [&](unsigned q) {
      const unsigned m = m0b + ((q + 3u) & 3u) * PAIR;
      if (q < 16) {
        const unsigned p = q & 7u, hb = (q & 8u) * SLOT;
        if (p == 0) HALF_K(hb, m); else HALF_V(hb + (p - 1u) * SLOT, m);
        if (p < 7) HALF_K(hb + (p + 1u) * SLOT, m + SLOT); else HALF_V(hb + 7u * SLOT, m + SLOT);
      } else {
        const unsigned s = q - 16u, hl = s == 0 ? 2u * gr + 1u : (s < 5 ? 2u * ((gr + s - 1u) & 3u) : 2u * ((gr + s - 4u) & 3u) + 1u);
        HALF_O(hl * 131072u, m); HALF_O(hl * 131072u + 65536u, m + SLOT);
      }
    };
    unsigned junk = 0;
#define TOUCH2(p, q) asm volatile("s_load_dword %0, %1, 0x0\n\ts_load_dword %0, %2, 0x0" : "+s"(junk) : "s"(p), "s"(q) : "memory")
    { const size_t o_ = ((size_t)b * 16 + hp) * 262144 + L * 131072;
      TOUCH2(K + o_, V + o_); TOUCH2(K + o_ + 65536, V + o_ + 65536);
      TOUCH2(WO + (size_t)gj * 1048576 + L * 1048572, (const char*)WB + L * WS_T);
      TOUCH2(OUT + (size_t)b * 131072 + L * 65536, OUT + (size_t)b * 131072 + L * 65536 + 65532); }
    { const u64 pc = (pc0 & ~255ull) + (u64)min((unsigned)(idx * 2 + L) * 512u, (unsigned)WARM_CODE - 512u);
      asm volatile("s_load_dword %0, %1, 0x0\n\ts_load_dword %0, %1, 0x40\n\ts_load_dword %0, %1, 0x80\n\ts_load_dword %0, %1, 0xc0\n\t"
                   "s_load_dword %0, %1, 0x100\n\ts_load_dword %0, %1, 0x140\n\ts_load_dword %0, %1, 0x180\n\ts_load_dword %0, %1, 0x1c0" : "+s"(junk) : "s"(pc) : "memory"); }
    waitvm<9>(); BAR();
    UNIT(1u, m0b + 3 * SLOT);
    asm volatile("s_waitcnt vmcnt(24)" : "+v"(fv) :: "memory");
    const u64 tag = (((u64)(unsigned)__builtin_amdgcn_readfirstlane((unsigned)(fv >> 32)) << 32) | (unsigned)__builtin_amdgcn_readfirstlane((unsigned)fv)) + 1ull;
    BAR();
    { unsigned m = m0b + 6 * SLOT;
      UNIT(2u, m);
#pragma unroll 1
      for (unsigned u = 1; u < 6; ++u) {
        m = (m == m0b + 6 * SLOT) ? m0b : m + 3 * SLOT;
        waitvm<24>(); BAR();
        UNIT(u + 2u, m);
      } }
    asm volatile("s_waitcnt lgkmcnt(0)" : "+s"(junk) :: "memory");
    STAMP(8);
    waitvm<24>(); BAR(); issue(0);
    waitvm<16>(); BAR(); issue(1);
#define WOPF 99
#define LA3END 10
#pragma unroll 1
    for (unsigned j = 0; j < 16; ++j) {
      if (j == 7) { waitvm<40>(); BAR(); }
      if (j == 15) { waitvm<24>(); BAR(); }
      if (j >= 1 && j <= LA3END) waitvm<32>(); else asm volatile("s_waitcnt vmcnt(16)" : "+v"(fv) :: "memory");
      if (j == 13) { if (!__all(fv == tag)) gwait(peerf, tag); }
      BAR();
      if (j == 12) FLOAD(fv, peerf);
      if (j == WOPF) {
#pragma unroll
        for (int i = 0; i < 4; ++i)
          asm volatile("buffer_load_dwordx4 %0, %1, %5, %6 offen\n\tbuffer_load_dwordx4 %0, %2, %5, %6 offen offset:1024\n\t"
                       "buffer_load_dwordx4 %0, %3, %5, %6 offen offset:2048\n\tbuffer_load_dwordx4 %0, %4, %5, %6 offen offset:3072"
                       : "+v"(keep) : "v"(vo[0]), "v"(vo[1]), "v"(vo[2]), "v"(vo[3]), "s"(ro), "s"((unsigned)bl * 131072u + (i >> 1) * 65536u + (i & 1) * 16384u) : "memory");
      }
      if (j == 0) issue(2u);
      if (j < LA3END) issue(j + 3u); else if (j > LA3END) issue(j + 2u);
    }
    issue(18u);
    STAMP(11);
#pragma unroll 1
    for (unsigned s = 0; s < 8; ++s) {
      if (s == 3) { asm volatile("s_waitcnt vmcnt(16)" : "+v"(fv) :: "memory"); if (!__all(fv == tag)) GSLOW(28, gwait(peerf + 256, tag)); }
      else if (s < 6) waitvm<32>(); else if (s == 6) waitvm<16>(); else waitvm<0>();
      BAR();
      if (s == 2) FLOAD(fv, peerf + 256);
      if (s < 5) issue(s + 19u);
    }
    asm volatile("" :: "v"(keep));
    STAMP(1); STAMP_DUMP();
    return;
  }
  STAMP(0);
  const int r32 = lane & 31, hi = lane >> 5;
  const unsigned kbase = lds0 + (r32 >> 3) * 2048 + (r32 & 7) * 64 + ((hi ^ ((r32 >> 2) & 3)) * 16);
  const unsigned vbase = lds0 + hi * 1024 + ((lane >> 2) & 3) * 64 + ((lane >> 4) & 1) * 32 + (lane & 3) * 8;
  u64 tag;
  { const u64 e = gld(WB + WS_E) + 1ull;
    tag = ((u64)(unsigned)__builtin_amdgcn_readfirstlane((unsigned)(e >> 32)) << 32) | (unsigned)__builtin_amdgcn_readfirstlane((unsigned)e); }
  const unsigned long long cy0 = CYC(); unsigned long long cyl = cy0; (void)cyl; unsigned wcyc = 1, wcnt = 1; (void)wcyc; (void)wcnt;
#define KRD(a0, a1, OFF, i) lds_b128(((((i) >> 1) & 1) ? (a1) : (a0)) + ((OFF) + 512 * ((i) >> 2) + ((i) & 1) * 8192))
#define VRD(a, OFF, i) lds_tr2((a) + ((OFF) + 4096 * ((i) >> 2) + 256 * ((i) & 3)), (a) + ((OFF) + 4096 * ((i) >> 2) + 256 * ((i) & 3) + 2048))
#define WRD(a, i) lds_tr2((a) + (4096 * ((i) >> 2) + 256 * ((i) & 3)), (a) + (4096 * ((i) >> 2) + 256 * ((i) & 3) + 1024))
#define RNEXT(a) (((a) + PAIR) & (RING - 1))
#define LBAR() do { __builtin_amdgcn_s_waitcnt(0xC07F); BAR(); } while (0)
#define WBAR() LBAR()
  bf16x8 qf[8], fr[36];
  unsigned ca, ka0, ka1;
  f32x16 qa[4];
  {
    const unsigned wbase = lds0 + hi * 2048 + ((lane >> 2) & 3) * 64 + ((lane >> 4) & 1) * 32 + (((lane & 1) << 1) | ((lane >> 1) & 1)) * 8;
    const unsigned xbase = lds0 + (wid * 32 + r32) * 128 + ((hi ^ ((r32 >> 1) & 7)) * 16);
    f32x16 acc[8];
#pragma unroll
    for (int d = 0; d < 8; ++d) { acc[d] = zero16(); PIN(acc[d]); }
    bf16x8 xa_[4], xb_[4], wf[38];
    asm volatile("" : "+s"(tag));
    BAR();
    ASTAMP(9);
    __builtin_amdgcn_s_waitcnt(0xC07F);
    unsigned uo = 0, xa = xbase; ca = wbase + SLOT;
    xa_[0] = lds_b128(xa); wf[1] = WRD(ca, 1); wf[0] = WRD(ca, 0); wf[3] = WRD(ca, 3); wf[2] = WRD(ca, 2);
    xa_[1] = lds_b128(xa ^ 32); wf[5] = WRD(ca, 5); wf[4] = WRD(ca, 4); xa_[2] = lds_b128(xa ^ 64); xa_[3] = lds_b128(xa ^ 96);
#define QUNIT(XC, XN, FIRST, LAST)                                                                                      \
    do {                                                                                                                \
      SBAR();                                                                                                           \
      _Pragma("unroll") for (int i = 0; i < 32; ++i) {                                                                  \
        const int ai = (i >> 4) * 4 + (i & 3);                                                                          \
        acc[ai] = MFMA(wf[i], XC[(i >> 2) & 3], acc[ai]);                                                               \
        if (i == 9) { if (FIRST) BAR(); }                                                                               \
        if (i == 25) { if (!(LAST)) { uo = (uo == 6 * SLOT) ? 0u : uo + 3 * SLOT; xa = xbase + uo; ca = wbase + SLOT + uo; PIN(xa); PIN(ca); } \
                       else { ka0 = kbase + 3 * PAIR; ka1 = ka0 ^ 32; PIN(ka0); PIN(ka1); } }        \
        if (i == ((LAST) ? 28 : 26)) { WBAR(); if (!(LAST)) XN[0] = lds_b128(xa); }                                     \
        if (!(LAST) && i == 27) { XN[1] = lds_b128(xa ^ 32); XN[2] = lds_b128(xa ^ 64); }                               \
        if (!(LAST) && i == 29) XN[3] = lds_b128(xa ^ 96);                                                              \
        if ((i & 1) == 0) { const int j = i + 6;                                                                        \
          if (j < 32 || !(LAST)) { wf[j + 1] = WRD(ca, (j + 1) & 31); wf[j] = WRD(ca, j & 31); }                        \
          else if (j >= 34) { fr[j - 33] = KRD(ka0, ka1, 0, j - 33); fr[j - 34] = KRD(ka0, ka1, 0, j - 34); } }         \
        if ((LAST) && i >= 17 && (i & 1)) { const int d_ = (i - 17) >> 1; qf[d_] = pack8(acc[d_ >> 1], d_ & 1, QSCALE); PIN(qf[d_]); }     \
        SBAR();                                                                                                         \
      }                                                                                                                 \
      if (!(LAST)) { _Pragma("unroll") for (int i = 0; i < 6; ++i) wf[i] = wf[32 + i]; }                                \
    } while (0)
    cyl = CYC();
#pragma unroll 1
    for (int D = 0; D < 3; ++D) { QUNIT(xa_, xb_, D == 0, false); QUNIT(xb_, xa_, false, false); }
    STAMPV(32, CYC() - cyl); STAMPV(33, wcyc); cyl = CYC();
    QUNIT(xa_, xb_, false, false); QUNIT(xb_, xa_, false, true);
    STAMPV(34, CYC() - cyl);
#pragma unroll
    for (int d = 0; d < 4; ++d) qa[d] = acc[4 + d];
    ca = vbase + 3 * PAIR;
  }
  ASTAMP(2); STAMPV(16, CYC() - cy0); STAMPV(18, wcyc); STAMPV(19, wcnt); wcyc = 1; wcnt = 1;
  bf16x8 sel;
  { const unsigned l_ = lane_id(); const short one = (((l_ >> 4) ^ (l_ >> 2)) & 1) ? (short)0 : (short)0x3F80;
#pragma unroll
    for (int i = 0; i < 8; ++i) sel[i] = one; }
  const rsrc_t rso = __builtin_amdgcn_make_buffer_rsrc((void*)(WB + WS_O + (gj * 8) * 32768 + wid * 8192), 0, 8 * 32768 - wid * 8192, 0x00020000);
  char* const fbase = WB + WS_F + wid * 64;
  bf16x8 of2[8], ob[8], oc[8];
  f32x16 sa[2], sb[2];
#define OLOAD(dst, hl) do { const unsigned vo_ = lane_id() * 16; _Pragma("unroll") for (int i_ = 0; i_ < 8; ++i_)        \
    dst[i_] = __builtin_bit_cast(bf16x8, __builtin_amdgcn_raw_buffer_load_b128(rso, vo_ + (i_ & 3) * 1024, (unsigned)(hl) * 32768u + (i_ >> 2) * 4096, SC1)); } while (0)
#define TILE0(PARK)                                                                                                     \
  do { const f32x16 zero = {}; const unsigned pa = lds0 + L_PARK + wid * 8192 + lane_id() * 16;                         \
    SBAR();                                                                                                             \
    _Pragma("unroll") for (int i = 0; i < 16; ++i) {                                                                    \
      sa[i & 1] = MFMA(fr[i], qf[i >> 1], i < 2 ? zero : sa[i & 1]);                                                    \
      if ((i & 1) == 0) { const int j = i + 4;                                                                          \
        if (j < 16) { fr[j + 1] = KRD(ka0, ka1, 0, j + 1); fr[j] = KRD(ka0, ka1, 0, j); } else { fr[j + 1] = KRD(ka0, ka1, SLOT, j - 15); fr[j] = KRD(ka0, ka1, SLOT, j - 16); } } \
      if ((PARK) && (i & 1)) { const int d_ = i >> 1; const bf16x8 w_ = pack8(qa[d_ >> 1], d_ & 1, QSCALE);             \
        *(LAS bf16x8*)(size_t)(pa + d_ * 1024) = w_; }                                                                  \
      SBAR();                                                                                                           \
    }                                                                                                                   \
    _Pragma("unroll") for (int i = 0; i < 4; ++i) fr[i] = fr[16 + i];                                                   \
  } while (0)
#define OFINISH(HD)                                                                                                     \
  do { const float inv = __builtin_amdgcn_rcpf(ls[0]);                                                                  \
    const unsigned vo_ = lane_id() * 16 + (unsigned)(2 * gr + (HD)) * 32768u;                                           \
    _Pragma("unroll") for (int i_ = 0; i_ < 8; ++i_) {                                                                  \
      of2[i_] = pack8(o[i_ >> 1], i_ & 1, inv);                                                                         \
      __builtin_amdgcn_raw_buffer_store_b128(__builtin_bit_cast(u32x4, of2[i_]), rso, vo_ + i_ * 1024, 0, SC1); }       \
  } while (0)
  f32x16 o[4];
  f32x4 ls;
  TILE0(true);
#pragma unroll 1
  for (int hd = 0;; ++hd) {
    f32x16 negm;
    ls = f32x4{0.f, 0.f, 0.f, 0.f};
    u32x4 pw[4];
    float fct = 1.f;
    bool pend = false;
    { const f32x16 zero = {};
      u32x4 z4 = {0u, 0u, 0u, 0u}; PIN(z4);
#pragma unroll
      for (int d = 0; d < 4; ++d) { o[d] = MFMA(__builtin_bit_cast(bf16x8, z4), __builtin_bit_cast(bf16x8, z4), zero); PIN(z4); }
      float m0 = max3f(sa[0][0], sa[0][1], sa[1][0]), m1 = max3f(sa[0][2], sa[0][3], sa[1][1]);
      m0 = max3f(m0, sa[1][2], sa[1][3]);
#pragma unroll
      for (int r = 4; r < 16; r += 4) { m0 = max3f(m0, sa[0][r], sa[0][r + 1]); m1 = max3f(m1, sa[0][r + 2], sa[0][r + 3]); m0 = max3f(m0, sa[1][r], sa[1][r + 1]); m1 = max3f(m1, sa[1][r + 2], sa[1][r + 3]); }
      float mx = fmaxf(m0, m1);
      auto rr = __builtin_amdgcn_permlane32_swap(__float_as_uint(mx), __float_as_uint(mx), false, false);
      const float mref = fmaxf(__uint_as_float(rr[0]), __uint_as_float(rr[1]));
#pragma unroll
      for (int i = 0; i < 16; ++i) { sa[0][i] = EXP2(sa[0][i] - mref); sa[1][i] -= mref; negm[i] = -mref; }
      PIN(sa[0]); PIN(sa[1]); PIN(negm);
    }
#define STEP(C, P, LAST)                                                                                                \
    do {                                                                                                                \
      SBAR();                                                                                                           \
      _Pragma("unroll") for (int i = 0; i < 16; ++i) {                                                                  \
        C[i & 1] = MFMA(fr[i], qf[i >> 1], i < 2 ? negm : C[i & 1]);                                                    \
        if (i == 11) { ca = RNEXT(ca); PIN(ca); }                                                                       \
        if (i == 12) WBAR();                                                                                            \
        if (i == 13) { ka0 = RNEXT(ka0); ka1 = ka0 ^ 32; PIN(ka0); PIN(ka1); }                                          \
        if ((i & 1) == 0) { const int j = i + 4;                                                                        \
          if (j < 16) { fr[j + 1] = KRD(ka0, ka1, SLOT, j + 1); fr[j] = KRD(ka0, ka1, SLOT, j); } else { fr[j + 1] = VRD(ca, 0, j - 15); fr[j] = VRD(ca, 0, j - 16); } } \
        { const int e0 = EA(i), e1 = EA(i + 1);                                                                         \
          _Pragma("unroll") for (int e = e0; e < e1; ++e) P[1][e] = EXP2(P[1][e]);                                      \
          pw[i >> 2][i & 3] = cvtpk(P[i >> 3][(2 * i) & 15], P[i >> 3][((2 * i) & 15) + 1]);                            \
          PIN(pw[i >> 2]); PIN(P[1]); }                                                                                 \
        SBAR();                                                                                                         \
      }                                                                                                                 \
      float m0 = 0.f, m1 = 0.f;                                                                                         \
      _Pragma("unroll") for (int i = 0; i < 16; ++i) {                                                                  \
        o[i & 3] = MFMA(fr[16 + i], __builtin_bit_cast(bf16x8, pw[i >> 2]), o[i & 3]);                                  \
        if ((LAST) && i == 12) WBAR();                                                                                  \
        if ((i & 1) == 0) { const int j = i + 4;                                                                        \
          if (j < 16) { fr[16 + j + 1] = VRD(ca, 0, j + 1); fr[16 + j] = VRD(ca, 0, j); }                               \
          else if (!(LAST)) { fr[16 + j + 1] = KRD(ka0, ka1, SLOT, j - 15); fr[16 + j] = KRD(ka0, ka1, SLOT, j - 16); } \
          else { fr[16 + j + 1] = VRD(ca, SLOT, j - 15); fr[16 + j] = VRD(ca, SLOT, j - 16); } }                        \
        if (i == 1) { m0 = max3f(C[0][0], C[0][1], C[0][2]); m1 = max3f(C[0][3], C[0][4], C[0][5]); m0 = max3f(m0, C[0][6], C[0][7]); m1 = max3f(m1, C[0][8], C[0][9]); \
                      m0 = max3f(m0, C[0][10], C[0][11]); m1 = max3f(m1, C[0][12], C[0][13]); PIN(m0); PIN(m1); }       \
        if (i == 2) { m0 = max3f(m0, C[0][14], C[0][15]); m1 = max3f(m1, C[1][0], C[1][1]); PIN(m0); PIN(m1); }         \
        if (i == 3) { m0 = max3f(m0, C[1][2], C[1][3]); m1 = max3f(m1, C[1][4], C[1][5]); m0 = max3f(m0, C[1][6], C[1][7]); m1 = max3f(m1, C[1][8], C[1][9]); \
                      m0 = max3f(m0, C[1][10], C[1][11]); m1 = max3f(m1, C[1][12], C[1][13]); PIN(m0); PIN(m1); }       \
        if (i == 4) { m0 = max3f(m0, C[1][14], C[1][15]); PIN(m0); }                                                    \
        if (i == 5) {                                                                                                   \
          if (__builtin_expect(__any((m0 > THR) | (m1 > THR)), 0)) {                                                    \
            const float mx = fmaxf(m0, m1);                                                                             \
            auto rr = __builtin_amdgcn_permlane32_swap(__float_as_uint(mx), __float_as_uint(mx), false, false);         \
            const float dl = fmaxf(fmaxf(__uint_as_float(rr[0]), __uint_as_float(rr[1])), 0.f);                         \
            fct = EXP2(-dl); pend = true;                                                                               \
            add_col3(C[0], C[1], negm, lane_id() < 32u ? 1.f : 0.f, -dl);                                               \
          } }                                                                                                           \
        if (i >= 6) { const int e0 = EB(i), e1 = EB(i + 1);                                                             \
          _Pragma("unroll") for (int e = e0; e < e1; ++e) C[0][e] = EXP2(C[0][e]);                                      \
          PIN(C[0]); }                                                                                                  \
        SBAR();                                                                                                         \
        if ((i & 3) == 3) { ls = MFMA16(sel, __builtin_bit_cast(bf16x8, pw[i >> 2]), ls); SBAR(); }                     \
      }                                                                                                                 \
      if (__builtin_expect(pend, 0)) {                                                                                  \
        _Pragma("unroll") for (int d = 0; d < 4; ++d) _Pragma("unroll") for (int e = 0; e < 16; ++e) o[d][e] *= fct;    \
        ls[0] *= fct; pend = false; }                                                                                   \
      _Pragma("unroll") for (int i = 0; i < 4; ++i) fr[i] = fr[32 + i];                                                 \
    } while (0)
#define EA(i) ((i) >= 11 ? 16 : ((i) / 2) * 3 + ((i) & 1))
#define EB(i) ((i) <= 6 ? 0 : ((i) >= 16 ? 16 : (((i) - 6) / 2) * 3 + (((i) - 6) & 1) + ((i) >= 13 ? 1 : 0)))
#pragma unroll 1
    for (int t = 1; t < 7; t += 2) {
      STEP(sb, sa, false);
      STEP(sa, sb, false);
      if (hd == 1 && t == 1) {
        waitvm<0>();
        if (lane_id() == 0) gst(fbase + hp * 768, tag);
      }
    }
    STEP(sb, sa, true);
    {
#pragma unroll
      for (int i = 0; i < 8; ++i) pw[i >> 2][i & 3] = cvtpk(sb[0][2 * i], sb[0][2 * i + 1]);
      SBAR();
#pragma unroll
      for (int i = 0; i < 16; ++i) {
        o[i & 3] = MFMA(fr[i], __builtin_bit_cast(bf16x8, pw[i >> 2]), o[i & 3]);
        if ((i & 1) == 0 && i + 4 < 16) { fr[i + 5] = VRD(ca, SLOT, i + 5); fr[i + 4] = VRD(ca, SLOT, i + 4); }
        if (i < 8) { sb[1][2 * i] = EXP2(sb[1][2 * i]); sb[1][2 * i + 1] = EXP2(sb[1][2 * i + 1]);
                     pw[2 + (i >> 2)][i & 3] = cvtpk(sb[1][2 * i], sb[1][2 * i + 1]); PIN(pw[2 + (i >> 2)]); }
        SBAR();
        if (i == 1 || i == 3 || i == 9 || i == 11) { ls = MFMA16(sel, __builtin_bit_cast(bf16x8, pw[(i >> 3) * 2 + ((i >> 1) & 1)]), ls); SBAR(); }
      }
      if (hd == 1) break;
      OFINISH(0);
      {
        const unsigned pa = lds0 + L_PARK + wid * 8192 + lane_id() * 16;
#pragma unroll
        for (int i = 0; i < 8; ++i) qf[i] = lds_b128(pa + i * 1024);
#pragma unroll
        for (int i = 0; i < 8; ++i) *(LAS bf16x8*)(size_t)(pa + i * 1024) = of2[i];
        WBAR();
        ka0 = RNEXT(ka0); ka1 = ka0 ^ 32; ca = RNEXT(ca);
        fr[1] = KRD(ka0, ka1, 0, 1); fr[0] = KRD(ka0, ka1, 0, 0); fr[3] = KRD(ka0, ka1, 0, 3); fr[2] = KRD(ka0, ka1, 0, 2);
        ASTAMP(3);
        TILE0(false);
      }
    }
  }
  OLOAD(ob, 2 * ((gr + 1) & 3)); OLOAD(oc, 2 * ((gr + 2) & 3));
  OFINISH(1);
  ASTAMP(4); STAMPV(17, CYC() - cy0); STAMPV(20, wcyc); STAMPV(21, wcnt); wcyc = 1; cyl = CYC();
  {
    const unsigned l_ = lane_id(), vo = l_ * 16;
    f32x16 acc[4];
    bf16x8 oa[8], wf[38];
#define FIDX(NC, i) ((NC) == 4 ? (i) : ((i) / 3) * 4 + 1 + (i) % 3)
#define OLD1(dst, hl, i_) dst[i_] = __builtin_bit_cast(bf16x8, __builtin_amdgcn_raw_buffer_load_b128(rso, vo + ((i_) & 3) * 1024, (unsigned)(hl) * 32768u + ((i_) >> 2) * 4096, SC1))
#define OPAIR(B, FIRST, NC, NN, MID)                                                                                    \
    do { const f32x16 zero = {};                                                                                        \
      SBAR();                                                                                                           \
      _Pragma("unroll") for (int i = 0; i < 8 * (NC); ++i) {                                                            \
        const int f_ = FIDX(NC, i);                                                                                     \
        acc[f_ & 3] = MFMA(wf[i], B[f_ >> 2], ((FIRST) && i < 4) ? zero : acc[f_ & 3]);                                 \
        if ((NN) && i == 8 * (NC) - 6) { WBAR(); ca = RNEXT(ca); PIN(ca); }                                             \
        { MID; }                                                                                                        \
        if ((i & 1) == 0) { const int j = i + 6;                                                                        \
          if (j < 8 * (NC)) { wf[j + 1] = VRD(ca, 0, FIDX(NC, j + 1)); wf[j] = VRD(ca, 0, FIDX(NC, j)); }               \
          else if (NN) { wf[j + 1] = VRD(ca, 0, FIDX(NN, j + 1 - 8 * (NC))); wf[j] = VRD(ca, 0, FIDX(NN, j - 8 * (NC))); } } \
        SBAR();                                                                                                         \
      }                                                                                                                 \
      if (NN) { _Pragma("unroll") for (int i = 0; i < 6; ++i) wf[i] = wf[8 * (NC) + i]; }                               \
    } while (0)
    WBAR();
    ca = RNEXT(ca);
#pragma unroll
    for (int i = 0; i < 6; i += 2) { wf[i + 1] = VRD(ca, 0, i + 1); wf[i] = VRD(ca, 0, i); }
    OPAIR(of2, true, 4, 4, { if (i == 19) { GSLOW(30, waitvm<0>()); if (l_ == 0) gst(fbase + hp * 768 + 256, tag); }
                             if ((i & 1) && i < 16) oa[i >> 1] = lds_b128(lds0 + L_PARK + wid * 8192 + vo + (i >> 1) * 1024); });
    OPAIR(oa, false, 4, 4, { if (i >= 12 && i < 20) OLD1(of2, 2 * ((gr + 3) & 3), (i - 12) & 7); });
    OPAIR(ob, false, 4, 4, {});
    ASTAMP(5);
    OPAIR(oc, false, 4, 3, { if (i >= 10 && i < 18) OLD1(oa, 2 * ((gr + 1) & 3) + 1, (i - 10) & 7); if (i >= 18 && i < 26) OLD1(ob, 2 * ((gr + 2) & 3) + 1, (i - 18) & 7); });
    ASTAMP(6);
    OPAIR(of2, false, 3, 3, { if (i >= 9 && i < 17) OLD1(oc, 2 * ((gr + 3) & 3) + 1, (i - 9) & 7); });
    OPAIR(oa, false, 3, 3, {});
    OPAIR(ob, false, 3, 3, {});
    OPAIR(oc, false, 3, 0, {});
    ASTAMP(7); STAMPV(25, CYC() - cyl);
    const rsrc_t rp = __builtin_amdgcn_make_buffer_rsrc((void*)(WB + WS_P + gr * 131072 + wid * 2048), 0, 131072 - wid * 2048, 0x00020000);
    { unsigned c4[4];
      c4[3] = ca; c4[0] = RNEXT(ca); c4[1] = RNEXT(c4[0]); c4[2] = RNEXT(c4[1]);
#pragma unroll
      for (int i = 0; i < 6; i += 2) { wf[i + 1] = VRD(c4[0], 0, 4 * (i + 1)); wf[i] = VRD(c4[0], 0, 4 * i); }
      SBAR();
#pragma unroll
      for (int i = 0; i < 32; ++i) {
        const bf16x8 bo = (i >> 3) == 0 ? of2[i & 7] : (i >> 3) == 1 ? oa[i & 7] : (i >> 3) == 2 ? ob[i & 7] : oc[i & 7];
        acc[0] = MFMA(wf[i], bo, acc[0]);
        if ((i & 1) == 0 && i + 6 < 32) { const int j = i + 6; wf[j + 1] = VRD(c4[(j + 1) >> 3], 0, 4 * ((j + 1) & 7)); wf[j] = VRD(c4[j >> 3], 0, 4 * (j & 7)); }
        if (i < 6) { const int s = 1 + (i >> 1);
          const unsigned va = vo + ((((unsigned)(gj + s) & 3u) * 4u + (unsigned)gj) * 8192u);
          __builtin_amdgcn_raw_buffer_store_b128(pack8n(acc[s], i & 1), rp, va + (i & 1) * 1024, 0, SC1); }
        if (i == 27) { GSLOW(31, waitvm<0>()); if (l_ == 0) gst(fbase + hp * 768 + 512, tag); }
        SBAR();
      } }
    f32x16 mine = acc[0];
    ASTAMP(10);
    GSLOW(29, gwait(fbase + (4 * (l_ & 3) + gr) * 768 + 512, tag));
    ASTAMP(13);
    u32x4 pp[3][2];
#pragma unroll
    for (int s = 0; s < 3; ++s) {
      const unsigned src = (unsigned)(gj + 1 + s) & 3u;
      pp[s][0] = __builtin_amdgcn_raw_buffer_load_b128(rp, vo, ((unsigned)gj * 4 + src) * 8192u, SC1);
      pp[s][1] = __builtin_amdgcn_raw_buffer_load_b128(rp, vo + 1024, ((unsigned)gj * 4 + src) * 8192u, SC1);
    }
    if (wid == 0 && hp == 0 && l_ == 0) gst(WB + WS_E, tag);
#pragma unroll
    for (int s = 0; s < 3; ++s)
#pragma unroll
      for (int i = 0; i < 8; ++i) {
        const unsigned w = pp[s][i >> 2][i & 3];
        mine[2 * i] += __uint_as_float(w << 16); mine[2 * i + 1] += __uint_as_float(w & 0xffff0000u);
      }
    u32x4 w0 = pack8n(mine, 0), w1 = pack8n(mine, 1);
    { auto r0 = __builtin_amdgcn_permlane32_swap(w0[0], w0[2], false, false); auto r1 = __builtin_amdgcn_permlane32_swap(w0[1], w0[3], false, false);
      w0 = u32x4{r0[0], r1[0], r0[1], r1[1]};
      auto r2 = __builtin_amdgcn_permlane32_swap(w1[0], w1[2], false, false); auto r3 = __builtin_amdgcn_permlane32_swap(w1[1], w1[3], false, false);
      w1 = u32x4{r2[0], r3[0], r2[1], r3[1]}; }
    char* dst = OUT + ((size_t)(b * 128 + wid * 32 + (l_ & 31)) * 512 + gr * 128 + gj * 32) * 2 + (l_ >> 5) * 16;
    *(u32x4*)dst = w0;
    *(u32x4*)(dst + 32) = w1;
  }
  STAMPV(59, CYC() - cy0); STAMP(14); STAMPV(23, wcyc); STAMPV(26, 1 + (__builtin_amdgcn_s_getreg((20 << 0) | (0 << 6) | (3 << 11)) & 15)); STAMP_DUMP();
}

extern "C" void kernel_launch(void* const* d_in, const int* in_sizes, int n_in, void* d_out, int out_size, void* d_ws, size_t ws_size, hipStream_t stream) {
  const char* x = (const char*)d_in[0]; const char* k = (const char*)d_in[1]; const char* v = (const char*)d_in[2];
  const char* wq = (const char*)d_in[3]; const char* wo = (const char*)d_in[4];
  (void)hipFuncSetAttribute((const void*)mha_fused, hipFuncAttributeMaxDynamicSharedMemorySize, (int)L_END);
  mha_fused<<<256, 384, L_END, stream>>>(x, k, v, wq, wo, (char*)d_ws, (char*)d_out);
}
```

```cpp
#define WARM_TLB
#define WARM_CODE 32768
#include <hip/hip_runtime.h>
#include <hip/hip_bf16.h>

typedef short    bf16x8 __attribute__((ext_vector_type(8)));
typedef short    s16x4  __attribute__((ext_vector_type(4)));
typedef float    f32x16 __attribute__((ext_vector_type(16)));
typedef float    f32x4  __attribute__((ext_vector_type(4)));
typedef float    f32x2  __attribute__((ext_vector_type(2)));
typedef __bf16   bf16x2 __attribute__((ext_vector_type(2)));
typedef unsigned u32x4  __attribute__((ext_vector_type(4)));
typedef unsigned u32x2  __attribute__((ext_vector_type(2)));
typedef unsigned long long u64;
typedef __amdgpu_buffer_rsrc_t rsrc_t;
#define LAS __attribute__((address_space(3)))
#define GAS __attribute__((address_space(1)))
#define DI __device__ __forceinline__
#define MA(D, A, B) asm volatile("v_mfma_f32_16x16x32_bf16 %0, %1, %2, %0" : "+v"(D) : "v"(A), "v"(B))
#define MC(D, A, B, C) asm volatile("v_mfma_f32_16x16x32_bf16 %0, %1, %2, %3" : "=&v"(D) : "v"(A), "v"(B), "v"(C))
#define MZ(D, A, B) asm volatile("v_mfma_f32_16x16x32_bf16 %0, %1, %2, 0" : "=&v"(D) : "v"(A), "v"(B))
#define NOPS(n) asm volatile("s_nop " #n)

constexpr float QSCALE = 0.08838834764831845f * 1.4426950408889634f;
constexpr float THR = 8.0f;

constexpr unsigned SLOT = 16384, PAIR = 32768, RING = 4 * PAIR;
constexpr unsigned L_PARK = RING, L_END = L_PARK + 32768;
constexpr size_t WS_B = 2u << 20;
constexpr size_t WS_O = 4096;
constexpr size_t WS_P = WS_O + (1u << 20);
constexpr size_t WS_F = WS_P + (512u << 10);
constexpr size_t WS_E = WS_F + 16384;
constexpr size_t WS_T = WS_E + 4096;

#define STAMP(i)
#define STAMPV(i, v)
#define STAMP_DECL
#define STAMP_DUMP()
#define CYC() 0ull
#define ASTAMP(i) STAMP(i)

DI unsigned cvtpk(float lo, float hi) { f32x2 v = {lo, hi}; bf16x2 b = __builtin_convertvector(v, bf16x2); return __builtin_bit_cast(unsigned, b); }
DI u32x4 pack2(const f32x4& t0, const f32x4& t1, float sc) { return u32x4{cvtpk(t0[0] * sc, t0[1] * sc), cvtpk(t0[2] * sc, t0[3] * sc), cvtpk(t1[0] * sc, t1[1] * sc), cvtpk(t1[2] * sc, t1[3] * sc)}; }
DI u32x4 pack2n(const f32x4& t0, const f32x4& t1) { return u32x4{cvtpk(t0[0], t0[1]), cvtpk(t0[2], t0[3]), cvtpk(t1[0], t1[1]), cvtpk(t1[2], t1[3])}; }
DI u32x4 natural(u32x4 w) {
  auto a0 = __builtin_amdgcn_permlane32_swap(w[0], w[2], false, false); auto a1 = __builtin_amdgcn_permlane32_swap(w[1], w[3], false, false);
  auto b0 = __builtin_amdgcn_permlane16_swap(a0[0], a0[1], false, false); auto b1 = __builtin_amdgcn_permlane16_swap(a1[0], a1[1], false, false);
  return u32x4{b0[0], b1[0], b0[1], b1[1]};
}
DI float allmax4(float m) {
  auto r = __builtin_amdgcn_permlane16_swap(__float_as_uint(m), __float_as_uint(m), false, false); m = fmaxf(__uint_as_float(r[0]), __uint_as_float(r[1]));
  auto q = __builtin_amdgcn_permlane32_swap(__float_as_uint(m), __float_as_uint(m), false, false); return fmaxf(__uint_as_float(q[0]), __uint_as_float(q[1]));
}
DI u32x4 lds_b128(unsigned a) { return *(const LAS u32x4*)(size_t)a; }
DI s16x4 lds_tr(unsigned a) { return __builtin_amdgcn_ds_read_tr16_b64_v4i16((LAS s16x4*)(size_t)a); }
#define CAT(lo, hi) __builtin_shufflevector(lo, hi, 0, 1, 2, 3, 4, 5, 6, 7)

template <int N> DI void waitvm() { asm volatile("s_waitcnt vmcnt(%0)" ::"i"(N) : "memory"); }
DI void dma4_nt(rsrc_t r, unsigned v0, unsigned v1, unsigned v2, unsigned v3, unsigned soff, unsigned m0v) {
  asm volatile("s_mov_b32 m0, %0\n\ts_nop 0\n\t"
               "buffer_load_dwordx4 %1, %5, %6 offen nt lds\n\t"
               "buffer_load_dwordx4 %2, %5, %6 offen offset:1024 nt lds\n\t"
               "buffer_load_dwordx4 %3, %5, %6 offen offset:2048 nt lds\n\t"
               "buffer_load_dwordx4 %4, %5, %6 offen offset:3072 nt lds" ::"s"(m0v), "v"(v0), "v"(v1), "v"(v2), "v"(v3), "s"(r), "s"(soff) : "memory");
}
DI void dma4(rsrc_t r, unsigned v0, unsigned v1, unsigned v2, unsigned v3, unsigned soff, unsigned m0v) {
  asm volatile("s_mov_b32 m0, %0\n\ts_nop 0\n\t"
               "buffer_load_dwordx4 %1, %5, %6 offen lds\n\t"
               "buffer_load_dwordx4 %2, %5, %6 offen offset:1024 lds\n\t"
               "buffer_load_dwordx4 %3, %5, %6 offen offset:2048 lds\n\t"
               "buffer_load_dwordx4 %4, %5, %6 offen offset:3072 lds" ::"s"(m0v), "v"(v0), "v"(v1), "v"(v2), "v"(v3), "s"(r), "s"(soff) : "memory");
}
constexpr unsigned GSPIN_MAX = 1u << 15;
#define BAR() asm volatile("s_barrier" ::: "memory")
#define SBAR() __builtin_amdgcn_sched_barrier(0)
#define PIN(x) asm volatile("" : "+v"(x))
DI float max3f(float a, float b, float c) { return __builtin_fmaxf(__builtin_fmaxf(a, b), c); }
DI void add_col5(f32x4& c0, f32x4& c1, f32x4& c2, f32x4& c3, f32x4& c4, float aone, float v) {
  asm volatile("s_nop 1\n\tv_mfma_f32_16x16x4_f32 %0, %5, %6, %0\n\tv_mfma_f32_16x16x4_f32 %1, %5, %6, %1\n\tv_mfma_f32_16x16x4_f32 %2, %5, %6, %2\n\t"
               "v_mfma_f32_16x16x4_f32 %3, %5, %6, %3\n\tv_mfma_f32_16x16x4_f32 %4, %5, %6, %4\n\ts_nop 15"
               : "+v"(c0), "+v"(c1), "+v"(c2), "+v"(c3), "+v"(c4) : "v"(aone), "v"(v));
}
DI unsigned lane_id() { unsigned l; asm volatile("v_mbcnt_lo_u32_b32 %0, -1, 0\n\tv_mbcnt_hi_u32_b32 %0, -1, %0" : "=v"(l)); return l; }
#define EXP2(x) __builtin_amdgcn_exp2f(x)
DI u64 gld(const char* p) { return __hip_atomic_load((const GAS u64*)p, __ATOMIC_RELAXED, __HIP_MEMORY_SCOPE_AGENT); }
DI void gst(char* p, u64 v) { __hip_atomic_store((GAS u64*)p, v, __ATOMIC_RELAXED, __HIP_MEMORY_SCOPE_AGENT); }
DI void gwait(const char* p, u64 tag) {
#pragma unroll 1
  for (unsigned s = 0; s < GSPIN_MAX; ++s) { if (__all(gld(p) == tag)) break; __builtin_amdgcn_s_sleep(2); }
  asm volatile("" ::: "memory");
}
constexpr int SC1 = 16;
#define SLOW(X) X
#define GSLOW(n, X) X

__global__ __launch_bounds__(384, 2) void mha_fused(const char* __restrict__ X, const char* __restrict__ K, const char* __restrict__ V,
                                                    const char* __restrict__ WQ, const char* __restrict__ WO, char* WS, char* __restrict__ OUT
) {
  extern __shared__ __attribute__((aligned(16))) char shm[];
  const unsigned lds0 = (unsigned)(size_t)(LAS char*)shm;
  const int tid = threadIdx.x, lane = tid & 63;
  const int wid = __builtin_amdgcn_readfirstlane(tid >> 6);
  const int g = blockIdx.x & 7, idx = blockIdx.x >> 3;
  const int bl = idx & 7, b = 8 * (g & 1) + bl, gj = g >> 1, gr = idx >> 3, hp = 4 * gj + gr;
  char* const WB = WS + (size_t)b * WS_B;
  u64 pc0; asm volatile("s_getpc_b64 %0" : "=s"(pc0));
  STAMP_DECL;
  if (wid >= 4) {
    const unsigned L = (unsigned)wid - 4u;
#define TBAR() BAR()
#define TISS()
    __builtin_amdgcn_s_setprio(3);
    const rsrc_t rx = __builtin_amdgcn_make_buffer_rsrc((void*)(X + (size_t)b * 131072 + L * 65536), 0, 131072 - L * 65536, 0x00020000);
    const rsrc_t rw = __builtin_amdgcn_make_buffer_rsrc((void*)(WQ + (size_t)hp * 512 + L * 262144), 0, 4194304 - hp * 512 - L * 262144, 0x00020000);
    const unsigned t4 = (lane >> 4) * 64 + (lane & 3) * 16, r4 = (lane >> 2) & 3;
    unsigned vx[4], vw[4];
#pragma unroll
    for (int j = 0; j < 4; ++j) {
      vx[j] = (lane >> 3) * 1024 + (((lane & 7) ^ ((4 * j + (lane >> 4)) & 7)) * 16) + j * (8192 - 1024);
      vw[j] = r4 * 8192 + (t4 ^ ((j >> 1) * 32)) + j * (32768 - 1024);
    }
    const unsigned rot = 2u * (unsigned)(gr + (bl >> 1)), sub = (unsigned)bl & 1u;
    const unsigned m0b = lds0 + L * 8192;
#define HALF_X(so, m) do { dma4(rx, vx[0], vx[1], vx[2], vx[3], (so), (m)); dma4(rx, vx[0], vx[1], vx[2], vx[3], (so) + 32768u, (m) + 4096u); } while (0)
#define HALF_W(so, m) do { dma4(rw, vw[0], vw[1], vw[2], vw[3], (so), (m)); dma4(rw, vw[0], vw[1], vw[2], vw[3], (so) + 131072u, (m) + 4096u); } while (0)
#define HALF_O(so, m) do { waitvm<52>(); dma4(ro, vo[0], vo[1], vo[2], vo[3], (so), (m)); dma4(ro, vo[0], vo[1], vo[2], vo[3], (so) + 16384u, (m) + 4096u); } while (0)
#define PACE 56
#define HALF_K(so, m) do { waitvm<PACE>(); dma4_nt(rk, vk[0], vk[1], vk[2], vk[3], (so), (m)); waitvm<PACE>(); dma4_nt(rk, vk[0], vk[1], vk[2], vk[3], (so) + 4096u, (m) + 4096u); } while (0)
#define HALF_V(so, m) do { waitvm<PACE>(); dma4_nt(rv, vv, vu, vv, vu, (so), (m)); waitvm<PACE>(); dma4_nt(rv, vv, vu, vv, vu, (so) + 4096u, (m) + 4096u); } while (0)
#define UNIT(u, m) do { const unsigned c_ = (((u) + rot) & 7u) ^ sub; HALF_X(c_ * 128u, (m)); HALF_W(c_ * 524288u, (m) + SLOT); HALF_W(c_ * 524288u + 256u, (m) + 2 * SLOT); } while (0)
#define FLOAD(dst, p) asm volatile("global_load_dwordx2 %0, %1, off sc1" : "=v"(dst) : "v"(p) : "memory")
    UNIT(0u, m0b);
    u64 fv;
    FLOAD(fv, WB + WS_E);
    const size_t kvb = ((size_t)b * 16 + hp) * 262144 + L * 8192;
    const rsrc_t rk = __builtin_amdgcn_make_buffer_rsrc((void*)(K + kvb), 0, 262144 - L * 8192, 0x00020000);
    const rsrc_t rv = __builtin_amdgcn_make_buffer_rsrc((void*)(V + kvb), 0, 262144 - L * 8192, 0x00020000);
    const rsrc_t ro = __builtin_amdgcn_make_buffer_rsrc((void*)(WO + (size_t)gj * 1048576 + gr * 256 + L * 32768), 0, 1048576 - gr * 256 - L * 32768, 0x00020000);
    unsigned vk[4], vo[4];
#pragma unroll
    for (int j = 0; j < 4; ++j) {
      vk[j] = (lane >> 4) * 256 + (((lane & 15) ^ (4 * j + (lane >> 4))) * 16);
      vo[j] = r4 * 1024 + (((lane >> 4) + gj) & 3) * 64 + (((lane & 3) * 16) ^ ((j & 1) * 32)) + j * (4096 - 1024);
    }
    const unsigned vv = r4 * 256 + t4, vu = vv ^ 32;
    const char* const peerf = WB + WS_F + (4 * gj + (lane & 3)) * 768 + ((lane >> 2) & 1) * 64;
    char* const myf = WB + WS_F + hp * 768 + L * 64;
    const rsrc_t rs = __builtin_amdgcn_make_buffer_rsrc((void*)(WB + WS_O + (size_t)(2 * hp) * 32768 + L * 16384), 0, 65536 - L * 16384, 0x00020000);
    auto ocopy = [&](unsigned hd) {
      u32x4 t[16];
#pragma unroll
      for (int i = 0; i < 16; ++i) t[i] = *(const LAS u32x4*)(size_t)(lds0 + L_PARK + L * 16384 + lane * 16 + i * 1024);
#pragma unroll
      for (int i = 0; i < 16; ++i) __builtin_amdgcn_raw_buffer_store_b128(t[i], rs, lane * 16 + hd * 32768u + i * 1024, 0, SC1);
      asm volatile("" ::: "memory");
    };
#define FSTORE(p) do { if (lane == 0) gst((p), tag); } while (0)
    auto issue = [&](unsigned q) {
      const unsigned m = m0b + ((q + 3u) & 3u) * PAIR;
      if (q < 16) {
        const unsigned p = q & 7u, hb = (q & 8u) * SLOT;
        if (p == 0) HALF_K(hb, m); else HALF_V(hb + (p - 1u) * SLOT, m);
        if (p < 7) HALF_K(hb + (p + 1u) * SLOT, m + SLOT); else HALF_V(hb + 7u * SLOT, m + SLOT);
      } else {
        const unsigned s = q - 16u, hl = s == 0 ? 2u * gr + 1u : (s < 5 ? 2u * ((gr + s - 1u) & 3u) : 2u * ((gr + s - 4u) & 3u) + 1u);
        HALF_O(hl * 131072u, m); HALF_O(hl * 131072u + 65536u, m + SLOT);
      }
    };
    unsigned junk = 0;
#define TOUCH2(p, q) asm volatile("s_load_dword %0, %1, 0x0\n\ts_load_dword %0, %2, 0x0" : "+s"(junk) : "s"(p), "s"(q) : "memory")
    { const size_t o_ = ((size_t)b * 16 + hp) * 262144 + L * 131072;
      TOUCH2(K + o_, V + o_); TOUCH2(K + o_ + 65536, V + o_ + 65536);
      TOUCH2(WO + (size_t)gj * 1048576 + L * 1048572, (const char*)WB + L * WS_T);
      TOUCH2(OUT + (size_t)b * 131072 + L * 65536, OUT + (size_t)b * 131072 + L * 65536 + 65532); }
    { const u64 pc = (pc0 & ~255ull) + (u64)min((unsigned)(idx * 2 + L) * 512u, (unsigned)WARM_CODE - 512u);
      asm volatile("s_load_dword %0, %1, 0x0\n\ts_load_dword %0, %1, 0x40\n\ts_load_dword %0, %1, 0x80\n\ts_load_dword %0, %1, 0xc0\n\t"
                   "s_load_dword %0, %1, 0x100\n\ts_load_dword %0, %1, 0x140\n\ts_load_dword %0, %1, 0x180\n\ts_load_dword %0, %1, 0x1c0" : "+s"(junk) : "s"(pc) : "memory"); }
    TISS(); waitvm<9>(); TBAR();
    UNIT(1u, m0b + 3 * SLOT);
    TISS(); asm volatile("s_waitcnt vmcnt(24)" : "+v"(fv) :: "memory");
    const u64 tag = (((u64)(unsigned)__builtin_amdgcn_readfirstlane((unsigned)(fv >> 32)) << 32) | (unsigned)__builtin_amdgcn_readfirstlane((unsigned)fv)) + 1ull;
    TBAR();
#define WOPF 2
    u32x4 keep = {0u, 0u, 0u, 0u};
    { unsigned m = m0b + 6 * SLOT;
      UNIT(2u, m);
#pragma unroll 1
      for (unsigned u = 1; u < 6; ++u) {
        m = (m == m0b + 6 * SLOT) ? m0b : m + 3 * SLOT;
        TISS(); if (u == WOPF + 1) waitvm<40>(); else waitvm<24>();
        TBAR();
        UNIT(u + 2u, m);
        if (u == WOPF) {
#pragma unroll
          for (int i = 0; i < 4; ++i)
            asm volatile("buffer_load_dwordx4 %0, %1, %5, %6 offen\n\tbuffer_load_dwordx4 %0, %2, %5, %6 offen offset:1024\n\t"
                         "buffer_load_dwordx4 %0, %3, %5, %6 offen offset:2048\n\tbuffer_load_dwordx4 %0, %4, %5, %6 offen offset:3072"
                         : "+v"(keep) : "v"(vo[0]), "v"(vo[1]), "v"(vo[2]), "v"(vo[3]), "s"(ro), "s"((unsigned)bl * 131072u + (i >> 1) * 65536u + (i & 1) * 16384u) : "memory");
        }
      } }
    asm volatile("s_waitcnt lgkmcnt(0)" : "+s"(junk) :: "memory");
    STAMP(8);
    TISS(); asm volatile("s_waitcnt vmcnt(24)" : "+v"(keep) :: "memory"); TBAR(); issue(0);
    TISS(); waitvm<16>(); TBAR(); issue(1);
#pragma unroll 1
    for (unsigned j = 0; j < 13; ++j) {
      TISS();
      if (j == 7) { waitvm<40>(); TBAR(); TISS(); }
      if (j == 9 || j == 10) waitvm<48>();
      else if (j >= 1 && j <= 8) waitvm<32>();
      else if (j == 12) waitvm<17>();
      else waitvm<16>();
      TBAR();
      if (j == 8) ocopy(0u);
      if (j == 11) FSTORE(myf);
      if (j == 0) issue(2u);
      if (j != 10) issue(j + (j < 10 ? 3u : 2u));
    }
    TISS(); waitvm<16>(); TBAR();
    FLOAD(fv, peerf); issue(15u);
    TISS(); waitvm<17>(); TBAR();
    issue(16u);
    TISS(); asm volatile("s_waitcnt vmcnt(24)" : "+v"(fv) :: "memory"); TBAR();
    TISS(); waitvm<16>();
    if (!__all(fv == tag)) gwait(peerf, tag);
    TBAR();
    issue(17u); issue(18u);
    STAMP(11);
    TISS(); waitvm<16>(); TBAR();
    ocopy(1u); issue(19u);
    TISS(); waitvm<16>(); FSTORE(myf + 256); TBAR();
    issue(20u); FLOAD(fv, peerf + 256); issue(21u);
    TISS(); asm volatile("s_waitcnt vmcnt(16)" : "+v"(fv) :: "memory");
    if (!__all(fv == tag)) GSLOW(28, gwait(peerf + 256, tag));
    TBAR();
    issue(22u);
    TISS(); waitvm<32>(); TBAR();
    issue(23u);
    TISS(); waitvm<32>(); TBAR();
    TISS(); waitvm<16>(); TBAR();
    TISS(); waitvm<0>(); TBAR();
    STAMP(1); STAMP_DUMP();
    return;
  }
  STAMP(0);
  u64 tag;
  { const u64 e = gld(WB + WS_E) + 1ull;
    tag = ((u64)(unsigned)__builtin_amdgcn_readfirstlane((unsigned)(e >> 32)) << 32) | (unsigned)__builtin_amdgcn_readfirstlane((unsigned)e); }
  const unsigned long long cy0 = CYC(); unsigned long long cyl = cy0; (void)cyl; unsigned wcyc = 1, wcnt = 1; (void)wcyc; (void)wcnt;
#define RNEXT(a) (((a) + PAIR) & (RING - 1))
#define LBAR() do { __builtin_amdgcn_s_waitcnt(0xC07F); BAR(); } while (0)
#define WBAR() LBAR()
#define KRD(kt, OFF, i) lds_b128((kt) + ((OFF) + 4096 * ((i) & 3)))
#define VRD(OFF, i, T) lds_tr((((i) & 1) ? ca1 : ca0) + ((OFF) + 8192 * ((i) >> 3) + 4096 * (T) + 256 * (((i) & 7) >> 1)))
#define WRD(i, T) lds_tr((((i) & 1) ? ca1 : ca0) + (8192 * ((i) >> 3) + 1024 * (T) + 256 * (((i) & 7) >> 1)))
  u32x4 qf[8], kf[16];
  s16x4 vl[38], vh[38];
  unsigned ca0, ca1, ka, kt;
  f32x4 qa[16];
  {
    const unsigned c16 = lane & 15, g4 = lane >> 4;
    const unsigned wbase = lds0 + SLOT + g4 * 2048 + ((lane >> 2) & 3) * 64 + (g4 & 1) * 32 + (lane & 3) * 8;
    const unsigned xbase = lds0 + (wid * 32 + c16) * 128 + ((g4 ^ (c16 >> 1)) * 16);
    f32x4 acc[32];
    { u32x4 z4 = {0u, 0u, 0u, 0u}; PIN(z4); NOPS(1);
#pragma unroll
      for (int d = 0; d < 32; ++d) MZ(acc[d], z4, z4); }
    u32x4 xa_[4], xb_[4];
    asm volatile("" : "+s"(tag));
    BAR();
    ASTAMP(9);
    __builtin_amdgcn_s_waitcnt(0xC07F);
    unsigned uo = 0, xa = xbase; ca0 = wbase; ca1 = ca0 ^ 32;
    xa_[1] = lds_b128(xa + 2048); xa_[0] = lds_b128(xa);
#pragma unroll
    for (int i = 0; i < 6; ++i) { vl[i ^ 1] = WRD(i ^ 1, 0); vh[i ^ 1] = WRD(i ^ 1, 1); }
    xa_[3] = lds_b128((xa ^ 64) + 2048); xa_[2] = lds_b128(xa ^ 64);
#define QUNIT(XC, XN, FIRST, LAST)                                                                                      \
    do {                                                                                                                \
      SBAR();                                                                                                           \
      _Pragma("unroll") for (int i = 0; i < 32; ++i) {                                                                  \
        const int ai = (i >> 4) * 16 + (i & 7) * 2, xi = ((i >> 3) & 1) * 2, j = (i ^ 1) + 6, n_ = 2 * (i - 28);        \
        MA(acc[ai], CAT(vl[i], vh[i]), XC[xi]); SBAR();                                                                 \
        if (i == 9) { if (FIRST) BAR(); }                                                                               \
        if (i == 28) WBAR();                                                                                            \
        if (j < 32) vl[j] = WRD(j, 0);                                                                                  \
        else if (i >= 28 && i < 31 && !(LAST)) { vl[33 + n_] = WRD(n_ + 1, 0); vh[33 + n_] = WRD(n_ + 1, 1); }          \
        else if (i >= 28 && (LAST)) kf[(i - 28) ^ 1] = KRD(ka, 0, (i - 28) ^ 1);                                        \
        if (i == 31 && !(LAST)) { XN[3] = lds_b128((xa ^ 64) + 2048); XN[2] = lds_b128(xa ^ 64); }                      \
        if (i == 26) { if (!(LAST)) { uo = (uo == 6 * SLOT) ? 0u : uo + 3 * SLOT; xa = xbase + uo; ca0 = wbase + uo; ca1 = ca0 ^ 32; PIN(xa); PIN(ca0); PIN(ca1); } \
                       else { const unsigned l_ = lane_id(); ka = lds0 + 3 * PAIR + (l_ & 15) * 256 + (((l_ >> 4) ^ (l_ & 15)) * 16); PIN(ka); } }        \
        if ((LAST) && i >= 18 && i < 26) { const int f_ = i - 18; qf[f_] = pack2(acc[(f_ >> 1) * 4 + (f_ & 1)], acc[(f_ >> 1) * 4 + 2 + (f_ & 1)], QSCALE); PIN(qf[f_]); } \
        SBAR();                                                                                                         \
        MA(acc[ai + 1], CAT(vl[i], vh[i]), XC[xi + 1]); SBAR();                                                         \
        if (j < 32) vh[j] = WRD(j, 1);                                                                                  \
        else if (i >= 28 && i < 31 && !(LAST)) { if (i == 28) { XN[1] = lds_b128(xa + 2048); XN[0] = lds_b128(xa); } vl[32 + n_] = WRD(n_, 0); vh[32 + n_] = WRD(n_, 1); } \
        if ((LAST) && i >= 19 && i < 27) { const int f_ = i - 19; qf[f_] = natural(qf[f_]); PIN(qf[f_]); }              \
        SBAR();                                                                                                         \
      }                                                                                                                 \
      if (!(LAST)) { _Pragma("unroll") for (int i = 0; i < 6; ++i) { vl[i] = vl[32 + i]; vh[i] = vh[32 + i]; } }        \
    } while (0)
    cyl = CYC();
#pragma unroll 1
    for (int D = 0; D < 3; ++D) { QUNIT(xa_, xb_, D == 0, false); QUNIT(xb_, xa_, false, false); }
    STAMPV(32, CYC() - cyl); STAMPV(33, wcyc); cyl = CYC();
    QUNIT(xa_, xb_, false, false); QUNIT(xb_, xa_, false, true);
    STAMPV(34, CYC() - cyl);
#pragma unroll
    for (int d = 0; d < 16; ++d) qa[d] = acc[16 + d];
  }
  ASTAMP(2); STAMPV(16, CYC() - cy0); STAMPV(18, wcyc); STAMPV(19, wcnt); wcyc = 1; wcnt = 1;
  { const unsigned l_ = lane_id(); ca0 = lds0 + 3 * PAIR + (l_ >> 4) * 1024 + ((l_ >> 2) & 3) * 64 + ((l_ >> 4) & 1) * 32 + (l_ & 3) * 8; ca1 = ca0 ^ 32; }
  u32x4 ones = {0x3F803F80u, 0x3F803F80u, 0x3F803F80u, 0x3F803F80u};
  PIN(ones);
  const rsrc_t rso = __builtin_amdgcn_make_buffer_rsrc((void*)(WB + WS_O + (gj * 8) * 32768 + wid * 8192), 0, 8 * 32768 - wid * 8192, 0x00020000);
  char* const fbase = WB + WS_F + wid * 64;
  u32x4 of2[8], oa[8], ob[8], oc[8];
  f32x4 sa[8], sb[8];
#define OLD1(dst, hl, i_) dst[i_] = __builtin_amdgcn_raw_buffer_load_b128(rso, vo + ((i_) & 3) * 1024, (unsigned)(hl) * 32768u + ((i_) >> 2) * 4096, SC1)
#define ST(e) ((((e) >> 4) * 2 + (((e) >> 2) & 1)) * 2 + (((e) >> 3) & 1))
#define SV(C, e) C[ST(e)][(e) & 3]
#define EXPV(C, e) do { SV(C, e) = EXP2(SV(C, e)); PIN(C[ST(e)]); } while (0)
#define PACKP(C, q) do { pw[(q) >> 2][(q) & 3] = cvtpk(SV(C, 2 * (q)), SV(C, 2 * (q) + 1)); PIN(pw[(q) >> 2]); } while (0)
#define TILE0(PARK)                                                                                                     \
  do { const unsigned pa = lds0 + L_PARK + wid * 8192 + lane_id() * 16; u32x4 w_;                                       \
    kt = ka ^ 64; PIN(kt); NOPS(1);                                                                                     \
    SBAR();                                                                                                             \
    _Pragma("unroll") for (int i = 0; i < 16; ++i) { const int kb = i & 3, s_ = i >> 2, j = (i ^ 1) + 4;                \
      if (s_ == 0) MZ(sa[kb * 2], kf[i], qf[0]); else MA(sa[kb * 2], kf[i], qf[s_ * 2]);                                \
      SBAR();                                                                                                           \
      if ((PARK) && (i & 1) == 0 && i >= 2) *(LAS u32x4*)(size_t)(pa + ((i >> 1) - 1) * 1024) = w_;                     \
      if ((PARK) && (i & 1) == 0) { const int f_ = i >> 1; w_ = pack2(qa[(f_ >> 1) * 4 + (f_ & 1)], qa[(f_ >> 1) * 4 + 2 + (f_ & 1)], QSCALE); PIN(w_); } \
      if ((PARK) && (i & 1)) { w_ = natural(w_); PIN(w_); }                                                             \
      SBAR();                                                                                                           \
      if (s_ == 0) MZ(sa[kb * 2 + 1], kf[i], qf[1]); else MA(sa[kb * 2 + 1], kf[i], qf[s_ * 2 + 1]);                    \
      SBAR();                                                                                                           \
      if (j < 16) kf[j] = KRD(kt, 0, j); else kf[j - 16] = KRD(ka, SLOT, j - 16);                                       \
      if (i == 3 || i == 7) { kt = ka ^ (64 * ((i + 5) >> 2)); PIN(kt); }                                               \
      SBAR();                                                                                                           \
    }                                                                                                                   \
    if (PARK) *(LAS u32x4*)(size_t)(pa + 7 * 1024) = w_;                                                                \
    kt = ka ^ 64; PIN(kt);                                                                                              \
  } while (0)
#define PINO() do { _Pragma("unroll") for (int d_ = 0; d_ < 16; ++d_) PIN(o[d_]); PIN(ls[0]); PIN(ls[1]); } while (0)
#define OFINISH(OLDV)                                                                                                   \
  do { NOPS(7); PINO();                                                                                                 \
    const float inv0 = __builtin_amdgcn_rcpf(ls[0][0]), inv1 = __builtin_amdgcn_rcpf(ls[1][0]);                         \
    const unsigned pa_ = lds0 + L_PARK + wid * 8192 + lane_id() * 16;                                                   \
    _Pragma("unroll") for (int i_ = 0; i_ < 8; ++i_) OLDV[i_] = lds_b128(pa_ + i_ * 1024);                              \
    _Pragma("unroll") for (int i_ = 0; i_ < 8; ++i_) {                                                                  \
      of2[i_] = pack2(o[(i_ >> 1) * 4 + (i_ & 1)], o[(i_ >> 1) * 4 + 2 + (i_ & 1)], (i_ & 1) ? inv1 : inv0);            \
      *(LAS u32x4*)(size_t)(pa_ + i_ * 1024) = of2[i_]; }                                                               \
  } while (0)
#define TMAX(C, qb) allmax4(fmaxf(max3f(max3f(max3f(C[qb][0], C[qb][1], C[qb][2]), C[qb][3], C[2 + qb][0]), C[2 + qb][1], max3f(C[2 + qb][2], C[2 + qb][3], C[4 + qb][0])), \
                                  max3f(max3f(C[4 + qb][1], C[4 + qb][2], C[4 + qb][3]), max3f(C[6 + qb][0], C[6 + qb][1], C[6 + qb][2]), C[6 + qb][3])))
  f32x4 o[16], ls[2];
  TILE0(true);
#pragma unroll 1
  for (int hd = 0;; ++hd) {
    f32x4 negm[2];
    u32x4 pw[4];
    float fct0 = 1.f, fct1 = 1.f;
    bool pend = false;
    { u32x4 z4 = {0u, 0u, 0u, 0u}; PIN(z4); NOPS(1);
#pragma unroll
      for (int d = 0; d < 16; ++d) MZ(o[d], z4, z4);
      MZ(ls[0], z4, z4); MZ(ls[1], z4, z4);
#pragma unroll
      for (int t = 0; t < 8; ++t) PIN(sa[t]);
      SBAR();
      const float m0 = TMAX(sa, 0), m1 = TMAX(sa, 1);
#pragma unroll
      for (int t = 0; t < 8; ++t)
#pragma unroll
        for (int r = 0; r < 4; ++r) { const float v = sa[t][r] - ((t & 1) ? m1 : m0); sa[t][r] = t < 4 ? EXP2(v) : v; }
#pragma unroll
      for (int r = 0; r < 4; ++r) { negm[0][r] = -m0; negm[1][r] = -m1; }
#pragma unroll
      for (int t = 0; t < 8; ++t) PIN(sa[t]);
      PIN(negm[0]); PIN(negm[1]); NOPS(1);
    }
#define CP(i) ((i) < 4 ? 0 : (i) > 11 ? 12 : (((i) - 4) * 3 + 1) / 2)
#define STEP(C, P, LAST)                                                                                                \
    do {                                                                                                                \
      SBAR();                                                                                                           \
      _Pragma("unroll") for (int i = 0; i < 16; ++i) { const int kb = i & 3, s_ = i >> 2, j = (i ^ 1) + 4, n_ = 2 * (i - 13); \
        if (s_ == 0) MC(C[kb * 2], kf[i], qf[0], negm[0]); else MA(C[kb * 2], kf[i], qf[s_ * 2]);                       \
        SBAR();                                                                                                         \
        if (i == 13) WBAR();                                                                                            \
        if (j < 16) kf[j] = KRD(kt, SLOT, j);                                                                           \
        else if (i == 13 || i == 14) { vl[n_ + 1] = VRD(0, n_ + 1, 0); vh[n_ + 1] = VRD(0, n_ + 1, 1); }                \
        SBAR();                                                                                                         \
        if (i < 4) EXPV(P, 16 + 2 * i); else if (i < 12) EXPV(P, 20 + i);                                               \
        if (i == 12) { PACKP(P, 12); PACKP(P, 13); ca0 = RNEXT(ca0); PIN(ca0); }                                        \
        if (i == 15) { PACKP(P, 15); ka = RNEXT(ka); PIN(ka); }                                                         \
        SBAR();                                                                                                         \
        if (s_ == 0) MC(C[kb * 2 + 1], kf[i], qf[1], negm[1]); else MA(C[kb * 2 + 1], kf[i], qf[s_ * 2 + 1]);           \
        SBAR();                                                                                                         \
        if (i == 13 || i == 14) { vl[n_] = VRD(0, n_, 0); vh[n_] = VRD(0, n_, 1); }                                     \
        SBAR();                                                                                                         \
        if (i < 4) EXPV(P, 17 + 2 * i);                                                                                 \
        _Pragma("unroll") for (int q = CP(i); q < CP(i + 1); ++q) PACKP(P, q);                                          \
        if (i == 12) { PACKP(P, 14); ca1 = ca0 ^ 32; PIN(ca1); }                                                        \
        if (i == 3 || i == 7) { kt = ka ^ (64 * ((i + 5) >> 2)); PIN(kt); }                                             \
        SBAR();                                                                                                         \
      }                                                                                                                 \
      float m0 = 0.f, m1 = 0.f;                                                                                         \
      _Pragma("unroll") for (int i = 0; i < 16; ++i) { const int e_ = i & 7, u_ = i >> 3, j = (i ^ 1) + 4, n_ = 2 * (i - 13); \
        MA(o[e_ * 2], CAT(vl[i], vh[i]), pw[u_ * 2]);                                                                   \
        SBAR();                                                                                                         \
        if ((LAST) && i == 13) WBAR();                                                                                  \
        if (j < 16) vl[j] = VRD(0, j, 0);                                                                               \
        else if (!(LAST)) kf[j - 16] = KRD(ka, SLOT, j - 16);                                                           \
        else if (i == 13 || i == 14) { vl[17 + n_] = VRD(SLOT, n_ + 1, 0); vh[17 + n_] = VRD(SLOT, n_ + 1, 1); }        \
        SBAR();                                                                                                         \
        if (i == 1) { m0 = max3f(C[0][0], C[0][1], C[0][2]); m1 = max3f(C[0][3], C[1][0], C[1][1]); PIN(m0); PIN(m1); } \
        if (i >= 2 && i <= 4) { m0 = max3f(m0, C[2 * i - 2][2], C[2 * i - 2][3]); m1 = max3f(m1, C[2 * i - 1][0], C[2 * i - 1][1]); PIN(m0); PIN(m1); } \
        if (i >= 6 && i < 14) EXPV(C, 2 * (i - 6));                                                                     \
        SBAR();                                                                                                         \
        MA(o[e_ * 2 + 1], CAT(vl[i], vh[i]), pw[u_ * 2 + 1]);                                                           \
        SBAR();                                                                                                         \
        if (j < 16) vh[j] = VRD(0, j, 1);                                                                               \
        else if ((LAST) && (i == 13 || i == 14)) { vl[16 + n_] = VRD(SLOT, n_, 0); vh[16 + n_] = VRD(SLOT, n_, 1); }    \
        SBAR();                                                                                                         \
        if (i >= 1 && i <= 3) { m0 = max3f(m0, C[2 * i - 1][2], C[2 * i - 1][3]); m1 = max3f(m1, C[2 * i][0], C[2 * i][1]); PIN(m0); PIN(m1); } \
        if (i == 4) { m0 = max3f(m0, C[7][2], C[7][3]); PIN(m0); }                                                      \
        if (i == 5) {                                                                                                   \
          if (__builtin_expect(__any((m0 > THR) | (m1 > THR)), 0)) {                                                    \
            const float d0 = fmaxf(TMAX(C, 0), 0.f), d1 = fmaxf(TMAX(C, 1), 0.f), a1 = lane_id() < 16u ? 1.f : 0.f;     \
            fct0 = EXP2(-d0); fct1 = EXP2(-d1); pend = true;                                                            \
            add_col5(C[0], C[2], C[4], C[6], negm[0], a1, -d0); add_col5(C[1], C[3], C[5], C[7], negm[1], a1, -d1);     \
          } }                                                                                                           \
        if (i >= 6 && i < 14) EXPV(C, 2 * (i - 6) + 1);                                                                 \
        if (i == 15 && !(LAST)) { kt = ka ^ 64; PIN(kt); }                                                              \
        SBAR();                                                                                                         \
        if ((i & 3) == 3) { MA(ls[(i >> 2) & 1], ones, pw[i >> 2]); SBAR(); }                                           \
      }                                                                                                                 \
      if (__builtin_expect(pend, 0)) { NOPS(7); PINO();                                                                       \
        _Pragma("unroll") for (int d = 0; d < 16; ++d) _Pragma("unroll") for (int e = 0; e < 4; ++e) o[d][e] *= (d & 1) ? fct1 : fct0;  \
        ls[0][0] *= fct0; ls[1][0] *= fct1; pend = false;                                                               \
        PINO(); NOPS(1); }                                                                              \
    } while (0)
#pragma unroll 1
    for (int t = 1; t < 7; t += 2) {
      STEP(sb, sa, false);
      STEP(sa, sb, false);
    }
    STEP(sb, sa, true);
    {
#pragma unroll
      for (int q = 0; q < 8; ++q) PACKP(sb, q);
      NOPS(1);
      SBAR();
#pragma unroll
      for (int i = 0; i < 16; ++i) { const int e_ = i & 7, u_ = i >> 3, j = (i ^ 1) + 4;
        MA(o[e_ * 2], CAT(vl[16 + i], vh[16 + i]), pw[u_ * 2]);
        SBAR();
        if (j < 16) vl[16 + j] = VRD(SLOT, j, 0);
        SBAR();
        if (i < 6) EXPV(sb, 16 + 3 * i);
        if (i < 5) EXPV(sb, 17 + 3 * i);
        if (i == 6) { PACKP(sb, 12); PACKP(sb, 13); }
        if (i == 7) { PACKP(sb, 15); }
        SBAR();
        MA(o[e_ * 2 + 1], CAT(vl[16 + i], vh[16 + i]), pw[u_ * 2 + 1]);
        SBAR();
        if (j < 16) vh[16 + j] = VRD(SLOT, j, 1);
        SBAR();
        if (i < 5) EXPV(sb, 18 + 3 * i);
        if (i >= 2 && i < 6) PACKP(sb, 6 + i);
        if (i == 6) PACKP(sb, 14);
        SBAR();
        if (i == 1 || i == 3 || i == 9 || i == 11) { MA(ls[(i >> 1) & 1], ones, pw[(i >> 3) * 2 + ((i >> 1) & 1)]); SBAR(); }
      }
      if (hd == 1) break;
      OFINISH(qf);
      WBAR();
      ka = RNEXT(ka); ca0 = RNEXT(ca0); ca1 = ca0 ^ 32;
      kf[1] = KRD(ka, 0, 1); kf[0] = KRD(ka, 0, 0); kf[3] = KRD(ka, 0, 3); kf[2] = KRD(ka, 0, 2);
      ASTAMP(3);
      TILE0(false);
    }
  }
  { const unsigned vo = lane_id() * 16;
#pragma unroll
    for (int i = 0; i < 8; ++i) OLD1(ob, 2 * ((gr + 1) & 3), i);
#pragma unroll
    for (int i = 0; i < 8; ++i) OLD1(oc, 2 * ((gr + 2) & 3), i); }
  OFINISH(oa);
  ASTAMP(4); STAMPV(17, CYC() - cy0); STAMPV(20, wcyc); STAMPV(21, wcnt); wcyc = 1; cyl = CYC();
  {
    const unsigned l_ = lane_id(), vo = l_ * 16;
    f32x4 acc[16];
#define FIDX(NC, i) ((NC) == 8 ? (i) : ((i) / 6) * 8 + 2 + (i) % 6)
#define OPAIR(B, FIRST, NC, NN, MID)                                                                                    \
    do {                                                                                                                \
      SBAR();                                                                                                           \
      _Pragma("unroll") for (int i = 0; i < 4 * (NC); ++i) {                                                            \
        const int f_ = FIDX(NC, i), N_ = 4 * (NC), j = (i ^ 1) + 6, n_ = 2 * (i - (N_ - 4));                            \
        if ((FIRST) && i < 8) MZ(acc[(f_ & 7) * 2], CAT(vl[i], vh[i]), B[0]); else MA(acc[(f_ & 7) * 2], CAT(vl[i], vh[i]), B[(f_ >> 3) * 2]); \
        SBAR();                                                                                                         \
        if ((NN) && i == N_ - 4 && !(FIRST)) WBAR();                                                                    \
        if (j < N_) vl[j] = VRD(0, FIDX(NC, j), 0);                                                                     \
        else if ((NN) && i >= N_ - 4 && i < N_ - 1) { vl[N_ + n_ + 1] = VRD(0, FIDX(NN, n_ + 1), 0); vh[N_ + n_ + 1] = VRD(0, FIDX(NN, n_ + 1), 1); } \
        if ((NN) && i == N_ - 6) { ca0 = RNEXT(ca0); ca1 = ca0 ^ 32; PIN(ca0); PIN(ca1); }                              \
        { MID; }                                                                                                        \
        SBAR();                                                                                                         \
        if ((FIRST) && i < 8) MZ(acc[(f_ & 7) * 2 + 1], CAT(vl[i], vh[i]), B[1]); else MA(acc[(f_ & 7) * 2 + 1], CAT(vl[i], vh[i]), B[(f_ >> 3) * 2 + 1]); \
        SBAR();                                                                                                         \
        if (j < N_) vh[j] = VRD(0, FIDX(NC, j), 1);                                                                     \
        else if ((NN) && i >= N_ - 4 && i < N_ - 1) { vl[N_ + n_] = VRD(0, FIDX(NN, n_), 0); vh[N_ + n_] = VRD(0, FIDX(NN, n_), 1); } \
        SBAR();                                                                                                         \
      }                                                                                                                 \
      if (NN) { _Pragma("unroll") for (int i = 0; i < 6; ++i) { vl[i] = vl[4 * (NC) + i]; vh[i] = vh[4 * (NC) + i]; } } \
    } while (0)
    WBAR();
    ca0 = RNEXT(ca0); ca1 = ca0 ^ 32;
#pragma unroll
    for (int i = 0; i < 6; ++i) { vl[i ^ 1] = VRD(0, i ^ 1, 0); vh[i ^ 1] = VRD(0, i ^ 1, 1); }
#define OFET(dst, hl) if (i % 3 == 1 && i < 24) OLD1(dst, hl, (i / 3) & 7)
    OPAIR(of2, true, 8, 8, {});
    OPAIR(oa, false, 8, 8, {});
    OPAIR(ob, false, 8, 8, { OFET(of2, 2 * ((gr + 3) & 3)); });
    ASTAMP(5);
    OPAIR(oc, false, 8, 6, { OFET(oa, 2 * ((gr + 1) & 3) + 1); });
    ASTAMP(6);
    OPAIR(of2, false, 6, 6, { OFET(ob, 2 * ((gr + 2) & 3) + 1); });
    OPAIR(oa, false, 6, 6, { OFET(oc, 2 * ((gr + 3) & 3) + 1); });
    OPAIR(ob, false, 6, 6, {});
    OPAIR(oc, false, 6, 0, {});
    ASTAMP(7); STAMPV(25, CYC() - cyl);
    const rsrc_t rp = __builtin_amdgcn_make_buffer_rsrc((void*)(WB + WS_P + gr * 131072 + wid * 2048), 0, 131072 - wid * 2048, 0x00020000);
    { unsigned c4[4];
      c4[3] = ca0; c4[0] = RNEXT(ca0); c4[1] = RNEXT(c4[0]); c4[2] = RNEXT(c4[1]);
#define DRD(i, T) lds_tr((c4[(i) >> 3] ^ (((i) & 1) * 32)) + (8192 * (((i) >> 1) & 3) + 4096 * (T)))
#pragma unroll
      for (int i = 0; i < 6; ++i) { vl[i ^ 1] = DRD(i ^ 1, 0); vh[i ^ 1] = DRD(i ^ 1, 1); }
      SBAR();
#pragma unroll
      for (int i = 0; i < 32; ++i) { const int j = (i ^ 1) + 6, k_ = ((i >> 1) & 3) * 2;
        const u32x4 b0 = (i >> 3) == 0 ? of2[k_] : (i >> 3) == 1 ? oa[k_] : (i >> 3) == 2 ? ob[k_] : oc[k_];
        const u32x4 b1 = (i >> 3) == 0 ? of2[k_ + 1] : (i >> 3) == 1 ? oa[k_ + 1] : (i >> 3) == 2 ? ob[k_ + 1] : oc[k_ + 1];
        MA(acc[(i & 1) * 2], CAT(vl[i], vh[i]), b0);
        SBAR();
        if (j < 32) vl[j] = DRD(j, 0);
        if (i < 6) { const int s = 1 + (i >> 1);
          const unsigned va = vo + ((((unsigned)(gj + s) & 3u) * 4u + (unsigned)gj) * 8192u);
          __builtin_amdgcn_raw_buffer_store_b128(pack2n(acc[4 * s + (i & 1)], acc[4 * s + 2 + (i & 1)]), rp, va + (i & 1) * 1024, 0, SC1); }
        if (i == 27) { GSLOW(31, waitvm<0>()); if (l_ == 0) gst(fbase + hp * 768 + 512, tag); }
        SBAR();
        MA(acc[(i & 1) * 2 + 1], CAT(vl[i], vh[i]), b1);
        SBAR();
        if (j < 32) vh[j] = DRD(j, 1);
        SBAR();
      } }
    ASTAMP(10);
    GSLOW(29, gwait(fbase + (4 * (l_ & 3) + gr) * 768 + 512, tag));
    ASTAMP(13);
    u32x4 pp[3][2];
#pragma unroll
    for (int s = 0; s < 3; ++s) {
      const unsigned src = (unsigned)(gj + 1 + s) & 3u;
      pp[s][0] = __builtin_amdgcn_raw_buffer_load_b128(rp, vo, ((unsigned)gj * 4 + src) * 8192u, SC1);
      pp[s][1] = __builtin_amdgcn_raw_buffer_load_b128(rp, vo + 1024, ((unsigned)gj * 4 + src) * 8192u, SC1);
    }
    if (wid == 0 && hp == 0 && l_ == 0) gst(WB + WS_E, tag);
#pragma unroll
    for (int qb = 0; qb < 2; ++qb) {
#pragma unroll
      for (int s = 0; s < 3; ++s)
#pragma unroll
        for (int w = 0; w < 4; ++w) {
          const unsigned x = pp[s][qb][w];
          acc[(w >> 1) * 2 + qb][(w & 1) * 2] += __uint_as_float(x << 16); acc[(w >> 1) * 2 + qb][(w & 1) * 2 + 1] += __uint_as_float(x & 0xffff0000u);
        }
      *(u32x4*)(OUT + ((size_t)(b * 128 + wid * 32 + qb * 16 + (l_ & 15)) * 512 + gr * 128 + gj * 32) * 2 + (l_ >> 4) * 16) = natural(pack2n(acc[qb], acc[2 + qb]));
    }
  }
  STAMPV(59, CYC() - cy0); STAMP(14); STAMPV(23, wcyc); STAMPV(26, 1 + (__builtin_amdgcn_s_getreg((20 << 0) | (0 << 6) | (3 << 11)) & 15)); STAMP_DUMP();
}

extern "C" void kernel_launch(void* const* d_in, const int* in_sizes, int n_in, void* d_out, int out_size, void* d_ws, size_t ws_size, hipStream_t stream) {
  const char* x = (const char*)d_in[0]; const char* k = (const char*)d_in[1]; const char* v = (const char*)d_in[2];
  const char* wq = (const char*)d_in[3]; const char* wo = (const char*)d_in[4];
  (void)hipFuncSetAttribute((const void*)mha_fused, hipFuncAttributeMaxDynamicSharedMemorySize, (int)L_END);
  mha_fused<<<256, 384, L_END, stream>>>(x, k, v, wq, wo, (char*)d_ws, (char*)d_out);
}
```

```cpp
#define WARM_TLB
#define WARM_CODE 32768
#include <hip/hip_runtime.h>
#include <hip/hip_bf16.h>

typedef short    bf16x8 __attribute__((ext_vector_type(8)));
typedef short    s16x4  __attribute__((ext_vector_type(4)));
typedef float    f32x16 __attribute__((ext_vector_type(16)));
typedef float    f32x4  __attribute__((ext_vector_type(4)));
typedef float    f32x2  __attribute__((ext_vector_type(2)));
typedef __bf16   bf16x2 __attribute__((ext_vector_type(2)));
typedef unsigned u32x4  __attribute__((ext_vector_type(4)));
typedef unsigned u32x2  __attribute__((ext_vector_type(2)));
typedef unsigned long long u64;
typedef __amdgpu_buffer_rsrc_t rsrc_t;
#define LAS __attribute__((address_space(3)))
#define GAS __attribute__((address_space(1)))
#define DI __device__ __forceinline__
#define MA(D, A, B) asm volatile("v_mfma_f32_16x16x32_bf16 %0, %1, %2, %0" : "+v"(D) : "v"(A), "v"(B))
#define MC(D, A, B, C) asm volatile("v_mfma_f32_16x16x32_bf16 %0, %1, %2, %3" : "=&v"(D) : "v"(A), "v"(B), "v"(C))
#define MZ(D, A, B) asm volatile("v_mfma_f32_16x16x32_bf16 %0, %1, %2, 0" : "=&v"(D) : "v"(A), "v"(B))
#define NOPS(n) asm volatile("s_nop " #n)

constexpr float QSCALE = 0.08838834764831845f * 1.4426950408889634f;
constexpr float THR = 8.0f;

constexpr unsigned SLOT = 16384, PAIR = 32768, RING = 4 * PAIR;
constexpr unsigned L_PARK = RING, L_END = L_PARK + 32768;
constexpr size_t WS_B = 2u << 20;
constexpr size_t WS_O = 4096;
constexpr size_t WS_P = WS_O + (1u << 20);
constexpr size_t WS_F = WS_P + (512u << 10);
constexpr size_t WS_E = WS_F + 16384;
constexpr size_t WS_T = WS_E + 4096;

#define STAMP(i)
#define STAMPV(i, v)
#define STAMP_DECL
#define STAMP_DUMP()
#define CYC() 0ull
#define ASTAMP(i) STAMP(i)

DI unsigned cvtpk(float lo, float hi) { f32x2 v = {lo, hi}; bf16x2 b = __builtin_convertvector(v, bf16x2); return __builtin_bit_cast(unsigned, b); }
DI u32x4 pack2(const f32x4& t0, const f32x4& t1, float sc) { return u32x4{cvtpk(t0[0] * sc, t0[1] * sc), cvtpk(t0[2] * sc, t0[3] * sc), cvtpk(t1[0] * sc, t1[1] * sc), cvtpk(t1[2] * sc, t1[3] * sc)}; }
DI u32x4 pack2n(const f32x4& t0, const f32x4& t1) { return u32x4{cvtpk(t0[0], t0[1]), cvtpk(t0[2], t0[3]), cvtpk(t1[0], t1[1]), cvtpk(t1[2], t1[3])}; }
DI u32x4 natural(u32x4 w) {
  auto a0 = __builtin_amdgcn_permlane32_swap(w[0], w[2], false, false); auto a1 = __builtin_amdgcn_permlane32_swap(w[1], w[3], false, false);
  auto b0 = __builtin_amdgcn_permlane16_swap(a0[0], a0[1], false, false); auto b1 = __builtin_amdgcn_permlane16_swap(a1[0], a1[1], false, false);
  return u32x4{b0[0], b1[0], b0[1], b1[1]};
}
DI float allmax4(float m) {
  auto r = __builtin_amdgcn_permlane16_swap(__float_as_uint(m), __float_as_uint(m), false, false); m = fmaxf(__uint_as_float(r[0]), __uint_as_float(r[1]));
  auto q = __builtin_amdgcn_permlane32_swap(__float_as_uint(m), __float_as_uint(m), false, false); return fmaxf(__uint_as_float(q[0]), __uint_as_float(q[1]));
}
DI u32x4 lds_b128(unsigned a) { return *(const LAS u32x4*)(size_t)a; }
DI s16x4 lds_tr(unsigned a) { return __builtin_amdgcn_ds_read_tr16_b64_v4i16((LAS s16x4*)(size_t)a); }
#define CAT(lo, hi) __builtin_shufflevector(lo, hi, 0, 1, 2, 3, 4, 5, 6, 7)

template <int N> DI void waitvm() { asm volatile("s_waitcnt vmcnt(%0)" ::"i"(N) : "memory"); }
DI void dma4_nt(rsrc_t r, unsigned v0, unsigned v1, unsigned v2, unsigned v3, unsigned soff, unsigned m0v) {
  asm volatile("s_mov_b32 m0, %0\n\ts_nop 0\n\t"
               "buffer_load_dwordx4 %1, %5, %6 offen nt lds\n\t"
               "buffer_load_dwordx4 %2, %5, %6 offen offset:1024 nt lds\n\t"
               "buffer_load_dwordx4 %3, %5, %6 offen offset:2048 nt lds\n\t"
               "buffer_load_dwordx4 %4, %5, %6 offen offset:3072 nt lds" ::"s"(m0v), "v"(v0), "v"(v1), "v"(v2), "v"(v3), "s"(r), "s"(soff) : "memory");
}
DI void dma4(rsrc_t r, unsigned v0, unsigned v1, unsigned v2, unsigned v3, unsigned soff, unsigned m0v) {
  asm volatile("s_mov_b32 m0, %0\n\ts_nop 0\n\t"
               "buffer_load_dwordx4 %1, %5, %6 offen lds\n\t"
               "buffer_load_dwordx4 %2, %5, %6 offen offset:1024 lds\n\t"
               "buffer_load_dwordx4 %3, %5, %6 offen offset:2048 lds\n\t"
               "buffer_load_dwordx4 %4, %5, %6 offen offset:3072 lds" ::"s"(m0v), "v"(v0), "v"(v1), "v"(v2), "v"(v3), "s"(r), "s"(soff) : "memory");
}
constexpr unsigned GSPIN_MAX = 1u << 15;
#define BAR() asm volatile("s_barrier" ::: "memory")
#define SBAR() __builtin_amdgcn_sched_barrier(0)
#define PIN(x) asm volatile("" : "+v"(x))
DI float max3f(float a, float b, float c) { return __builtin_fmaxf(__builtin_fmaxf(a, b), c); }
DI void add_col5(f32x4& c0, f32x4& c1, f32x4& c2, f32x4& c3, f32x4& c4, float aone, float v) {
  asm volatile("s_nop 1\n\tv_mfma_f32_16x16x4_f32 %0, %5, %6, %0\n\tv_mfma_f32_16x16x4_f32 %1, %5, %6, %1\n\tv_mfma_f32_16x16x4_f32 %2, %5, %6, %2\n\t"
               "v_mfma_f32_16x16x4_f32 %3, %5, %6, %3\n\tv_mfma_f32_16x16x4_f32 %4, %5, %6, %4\n\ts_nop 15"
               : "+v"(c0), "+v"(c1), "+v"(c2), "+v"(c3), "+v"(c4) : "v"(aone), "v"(v));
}
DI unsigned lane_id() { unsigned l; asm volatile("v_mbcnt_lo_u32_b32 %0, -1, 0\n\tv_mbcnt_hi_u32_b32 %0, -1, %0" : "=v"(l)); return l; }
#define EXP2(x) __builtin_amdgcn_exp2f(x)
DI u64 gld(const char* p) { return __hip_atomic_load((const GAS u64*)p, __ATOMIC_RELAXED, __HIP_MEMORY_SCOPE_AGENT); }
DI void gst(char* p, u64 v) { __hip_atomic_store((GAS u64*)p, v, __ATOMIC_RELAXED, __HIP_MEMORY_SCOPE_AGENT); }
DI void gwait(const char* p, u64 tag) {
#pragma unroll 1
  for (unsigned s = 0; s < GSPIN_MAX; ++s) { if (__all(gld(p) == tag)) break; __builtin_amdgcn_s_sleep(2); }
  asm volatile("" ::: "memory");
}
#define BSEL (g & 1)
#define GSEL (g >> 1)
#define PLAIN1 0
#define PLAIN2 0
constexpr int SC1 = 16;
#define SLOW(X) X
#define GSLOW(n, X) X

__global__ __launch_bounds__(384, 2) void mha_fused(const char* X, const char* K, const char* V,
                                                    const char* WQ, const char* WO, char* WS, char* OUT
) {
  extern __shared__ __attribute__((aligned(16))) char shm[];
  const unsigned lds0 = (unsigned)(size_t)(LAS char*)shm;
  asm volatile("" : "+s"(X), "+s"(K), "+s"(V), "+s"(WQ), "+s"(WO), "+s"(WS), "+s"(OUT));
  const unsigned long long cyk = CYC(); (void)cyk;
  const int tid = threadIdx.x, lane = tid & 63;
  const int wid = __builtin_amdgcn_readfirstlane(tid >> 6);
  const int g = blockIdx.x & 7, idx = blockIdx.x >> 3;
  const int bl = idx & 7, b = 8 * (BSEL) + bl, gj = GSEL, gr = idx >> 3, hp = 4 * gj + gr;
  char* const WB = WS + (size_t)b * WS_B;
  u64 pc0; asm volatile("s_getpc_b64 %0" : "=s"(pc0));
  STAMP_DECL;
  if (wid >= 4) {
    const unsigned L = (unsigned)wid - 4u;
#define TBAR() BAR()
#define TISS()
    __builtin_amdgcn_s_setprio(3);
    STAMPV(41, CYC() - cyk);
    const rsrc_t rx = __builtin_amdgcn_make_buffer_rsrc((void*)(X + (size_t)b * 131072 + L * 65536), 0, 131072 - L * 65536, 0x00020000);
    const rsrc_t rw = __builtin_amdgcn_make_buffer_rsrc((void*)(WQ + (size_t)hp * 512 + L * 262144), 0, 4194304 - hp * 512 - L * 262144, 0x00020000);
    const unsigned t4 = (lane >> 4) * 64 + (lane & 3) * 16, r4 = (lane >> 2) & 3;
    unsigned vx[4], vw[4];
#pragma unroll
    for (int j = 0; j < 4; ++j) {
      vx[j] = (lane >> 3) * 1024 + (((lane & 7) ^ ((4 * j + (lane >> 4)) & 7)) * 16) + j * (8192 - 1024);
      vw[j] = r4 * 8192 + (t4 ^ ((j >> 1) * 32)) + j * (32768 - 1024);
    }
    const unsigned rot = 2u * (unsigned)(gr + (bl >> 1)), sub = (unsigned)bl & 1u;
    const unsigned m0b = lds0 + L * 8192;
#define HALF_X(so, m) do { dma4(rx, vx[0], vx[1], vx[2], vx[3], (so), (m)); dma4(rx, vx[0], vx[1], vx[2], vx[3], (so) + 32768u, (m) + 4096u); } while (0)
#define HALF_W(so, m) do { dma4(rw, vw[0], vw[1], vw[2], vw[3], (so), (m)); dma4(rw, vw[0], vw[1], vw[2], vw[3], (so) + 131072u, (m) + 4096u); } while (0)
#define HALF_O(so, m) do { waitvm<52>(); dma4(ro, vo[0], vo[1], vo[2], vo[3], (so), (m)); dma4(ro, vo[0], vo[1], vo[2], vo[3], (so) + 16384u, (m) + 4096u); } while (0)
#define PACE 56
#define HALF_K(so, m) do { waitvm<PACE>(); dma4_nt(rk, vk[0], vk[1], vk[2], vk[3], (so), (m)); waitvm<PACE>(); dma4_nt(rk, vk[0], vk[1], vk[2], vk[3], (so) + 4096u, (m) + 4096u); } while (0)
#define HALF_V(so, m) do { waitvm<PACE>(); dma4_nt(rv, vv, vu, vv, vu, (so), (m)); waitvm<PACE>(); dma4_nt(rv, vv, vu, vv, vu, (so) + 4096u, (m) + 4096u); } while (0)
#define UNIT(u, m) do { const unsigned c_ = (((u) + rot) & 7u) ^ sub; HALF_X(c_ * 128u, (m)); HALF_W(c_ * 524288u, (m) + SLOT); HALF_W(c_ * 524288u + 256u, (m) + 2 * SLOT); } while (0)
#define FLOAD(dst, p) asm volatile("global_load_dwordx2 %0, %1, off sc1" : "=v"(dst) : "v"(p) : "memory")
    { const unsigned c_ = (rot & 7u) ^ sub; HALF_W(c_ * 524288u + 256u, m0b + 2 * SLOT); }
    u64 fv;
    { const unsigned c_ = ((1u + rot) & 7u) ^ sub; HALF_X(c_ * 128u, m0b + 3 * SLOT); }
    const size_t kvb = ((size_t)b * 16 + hp) * 262144 + L * 8192;
    const rsrc_t rk = __builtin_amdgcn_make_buffer_rsrc((void*)(K + kvb), 0, 262144 - L * 8192, 0x00020000);
    const rsrc_t rv = __builtin_amdgcn_make_buffer_rsrc((void*)(V + kvb), 0, 262144 - L * 8192, 0x00020000);
    const rsrc_t ro = __builtin_amdgcn_make_buffer_rsrc((void*)(WO + (size_t)gj * 1048576 + gr * 256 + L * 32768), 0, 1048576 - gr * 256 - L * 32768, 0x00020000);
    unsigned vk[4], vo[4];
#pragma unroll
    for (int j = 0; j < 4; ++j) {
      vk[j] = (lane >> 4) * 256 + (((lane & 15) ^ (4 * j + (lane >> 4))) * 16);
      vo[j] = r4 * 1024 + (((lane >> 4) + gj) & 3) * 64 + (((lane & 3) * 16) ^ ((j & 1) * 32)) + j * (4096 - 1024);
    }
    const unsigned vv = r4 * 256 + t4, vu = vv ^ 32;
    const char* const peerf = WB + WS_F + (4 * gj + (lane & 3)) * 768 + ((lane >> 2) & 1) * 64;
    char* const myf = WB + WS_F + hp * 768 + L * 64;
    const rsrc_t rs = __builtin_amdgcn_make_buffer_rsrc((void*)(WB + WS_O + (size_t)(2 * hp) * 32768 + L * 16384), 0, 65536 - L * 16384, 0x00020000);
    auto ocopy = [&](unsigned hd, bool plain) {
      u32x4 t[16];
#pragma unroll
      for (int i = 0; i < 16; ++i) t[i] = *(const LAS u32x4*)(size_t)(lds0 + L_PARK + L * 16384 + lane * 16 + i * 1024);
#pragma unroll
      for (int i = 0; i < 16; ++i) { if (plain) __builtin_amdgcn_raw_buffer_store_b128(t[i], rs, lane * 16 + hd * 32768u + i * 1024, 0, 0);
                                     else __builtin_amdgcn_raw_buffer_store_b128(t[i], rs, lane * 16 + hd * 32768u + i * 1024, 0, SC1); }
      asm volatile("" ::: "memory");
    };
#define FSTORE(p) do { if (lane == 0) gst((p), tag); } while (0)
    auto issue = [&](unsigned q) {
      const unsigned m = m0b + ((q + 3u) & 3u) * PAIR;
      if (q < 16) {
        const unsigned p = q & 7u, hb = (q & 8u) * SLOT;
        if (p == 0) HALF_K(hb, m); else HALF_V(hb + (p - 1u) * SLOT, m);
        if (p < 7) HALF_K(hb + (p + 1u) * SLOT, m + SLOT); else HALF_V(hb + 7u * SLOT, m + SLOT);
      } else {
        const unsigned s = q - 16u, hl = s == 0 ? 2u * gr + 1u : (s < 5 ? 2u * ((gr + s - 1u) & 3u) : 2u * ((gr + s - 4u) & 3u) + 1u);
        HALF_O(hl * 131072u, m); HALF_O(hl * 131072u + 65536u, m + SLOT);
      }
    };
    STAMPV(40, CYC() - cyk);
    unsigned junk = 0;
#define TOUCH2(p, q) asm volatile("s_load_dword %0, %1, 0x0\n\ts_load_dword %0, %2, 0x0" : "+s"(junk) : "s"(p), "s"(q) : "memory")
    { const size_t o_ = ((size_t)b * 16 + hp) * 262144 + L * 131072;
      TOUCH2(K + o_, V + o_); TOUCH2(K + o_ + 65536, V + o_ + 65536);
      TOUCH2(WO + (size_t)gj * 1048576 + L * 1048572, (const char*)WB + L * WS_T);
      TOUCH2(OUT + (size_t)b * 131072 + L * 65536, OUT + (size_t)b * 131072 + L * 65536 + 65532); }
    { const u64 pc = (pc0 & ~255ull) + (u64)min((unsigned)(idx * 2 + L) * 512u, (unsigned)WARM_CODE - 512u);
      asm volatile("s_load_dword %0, %1, 0x0\n\ts_load_dword %0, %1, 0x40\n\ts_load_dword %0, %1, 0x80\n\ts_load_dword %0, %1, 0xc0\n\t"
                   "s_load_dword %0, %1, 0x100\n\ts_load_dword %0, %1, 0x140\n\ts_load_dword %0, %1, 0x180\n\ts_load_dword %0, %1, 0x1c0" : "+s"(junk) : "s"(pc) : "memory"); }
    STAMPV(39, CYC() - cyk);
    TISS(); TBAR();
    { const unsigned c_ = ((1u + rot) & 7u) ^ sub; HALF_W(c_ * 524288u, m0b + 4 * SLOT); HALF_W(c_ * 524288u + 256u, m0b + 5 * SLOT); }
    TISS(); waitvm<24>(); TBAR();
    u32x4 keep = {0u, 0u, 0u, 0u}, ra[16], rb[16], rc[16]; (void)keep; (void)ra; (void)rb; (void)rc;
#define RL4(R, i, rs_, v0, v1, v2, v3, so)                                                                              \
    asm volatile("buffer_load_dwordx4 %0, %4, %8, %9 offen nt\n\tbuffer_load_dwordx4 %1, %5, %8, %9 offen offset:1024 nt\n\t" \
                 "buffer_load_dwordx4 %2, %6, %8, %9 offen offset:2048 nt\n\tbuffer_load_dwordx4 %3, %7, %8, %9 offen offset:3072 nt" \
                 : "=&v"(R[i]), "=&v"(R[(i) + 1]), "=&v"(R[(i) + 2]), "=&v"(R[(i) + 3]) : "v"(v0), "v"(v1), "v"(v2), "v"(v3), "s"(rs_), "s"(so) : "memory")
#define RLOAD0(R, q) do { const unsigned sv_ = ((q) - 1u) * SLOT; RL4(R, 0, rv, vv, vu, vv, vu, sv_); RL4(R, 4, rv, vv, vu, vv, vu, sv_ + 4096u); } while (0)
#define RLOAD1(R, q) do { const unsigned sk_ = ((q) + 1u) * SLOT; RL4(R, 8, rk, vk[0], vk[1], vk[2], vk[3], sk_); RL4(R, 12, rk, vk[0], vk[1], vk[2], vk[3], sk_ + 4096u); } while (0)
#define RW4(R, i, a_, o_) asm volatile("ds_write_b128 %0, %1 offset:%5\n\tds_write_b128 %0, %2 offset:%6\n\tds_write_b128 %0, %3 offset:%7\n\tds_write_b128 %0, %4 offset:%8" \
                 :: "v"(a_), "v"(R[i]), "v"(R[(i) + 1]), "v"(R[(i) + 2]), "v"(R[(i) + 3]), "i"(o_), "i"((o_) + 1024), "i"((o_) + 2048), "i"((o_) + 3072) : "memory")
#define RWRITE(R, q) do { const unsigned a_ = m0b + (((q) + 3u) & 3u) * PAIR + lane * 16;                               \
      RW4(R, 0, a_, 0); RW4(R, 4, a_, 4096); RW4(R, 8, a_, 16384); RW4(R, 12, a_, 20480); } while (0)
#define WAITL() asm volatile("s_waitcnt lgkmcnt(0)" ::: "memory")
#define UNITW(u, r) do { waitvm<38>(); UNIT(u, m0b + (r) * 3 * SLOT); } while (0)
#define WOPREF() do { _Pragma("unroll") for (int i = 0; i < 4; ++i)                                                     \
        asm volatile("buffer_load_dwordx4 %0, %1, %5, %6 offen\n\tbuffer_load_dwordx4 %0, %2, %5, %6 offen offset:1024\n\t" \
                     "buffer_load_dwordx4 %0, %3, %5, %6 offen offset:2048\n\tbuffer_load_dwordx4 %0, %4, %5, %6 offen offset:3072" \
                     : "+v"(keep) : "v"(vo[0]), "v"(vo[1]), "v"(vo[2]), "v"(vo[3]), "s"(ro), "s"((unsigned)bl * 131072u + (i >> 1) * 65536u + (i & 1) * 16384u) : "memory"); } while (0)
    UNITW(2u, 2);
    TISS(); waitvm<24>(); TBAR();
    UNITW(3u, 0);
    TISS(); waitvm<24>(); TBAR();
    UNITW(4u, 1);
    waitvm<44>(); WOPREF();
    TISS(); waitvm<40>(); TBAR();
    UNITW(5u, 2);
    TISS(); waitvm<40>(); TBAR();
    UNITW(6u, 0);
    TISS(); waitvm<24>(); TBAR();
    UNITW(7u, 1);
    asm volatile("s_waitcnt lgkmcnt(0)" : "+s"(junk) :: "memory"); STAMP(8);
    TISS(); waitvm<24>(); TBAR();
    FLOAD(fv, WB + WS_E);
    issue(0u);
    TISS(); asm volatile("s_waitcnt vmcnt(17)" : "+v"(keep) :: "memory"); TBAR();
    issue(1u);
    asm volatile("s_waitcnt vmcnt(32)" : "+v"(fv) :: "memory");
    const u64 tag = (((u64)(unsigned)__builtin_amdgcn_readfirstlane((unsigned)(fv >> 32)) << 32) | (unsigned)__builtin_amdgcn_readfirstlane((unsigned)fv)) + 1ull;
    TISS(); waitvm<16>(); TBAR();
    issue(2u);
    issue(3u);
    TISS(); waitvm<32>(); TBAR();
    issue(4u);
    TISS(); waitvm<32>(); TBAR();
    issue(5u);
    TISS(); waitvm<32>(); TBAR();
    issue(6u);
    TISS(); waitvm<32>(); TBAR();
    issue(7u);
    TISS(); waitvm<32>(); TBAR();
    issue(8u);
    TISS(); waitvm<32>(); TBAR();
    issue(9u);
    TISS(); waitvm<40>(); TBAR();
    TISS(); waitvm<32>(); TBAR();
    issue(10u);
    TISS(); waitvm<32>(); TBAR();
    waitvm<44>(); ocopy(0u, false);
    issue(11u);
    TISS(); waitvm<48>(); TBAR();
    issue(12u);
    TISS(); waitvm<48>(); TBAR();
    TISS(); waitvm<16>(); FSTORE(myf); TBAR();
    issue(13u);
    TISS(); waitvm<17>(); TBAR();
    issue(14u);
    TISS(); waitvm<16>(); TBAR();
    FLOAD(fv, peerf);
    issue(15u);
    TISS(); waitvm<17>(); TBAR();
    issue(16u);
    TISS(); asm volatile("s_waitcnt vmcnt(24)" : "+v"(fv) :: "memory"); TBAR();
    TISS(); asm volatile("s_waitcnt vmcnt(16)" : "+v"(fv) :: "memory"); if (!__all(fv == tag)) gwait(peerf, tag); TBAR();
    issue(17u);
    issue(18u);
    STAMP(11);
    TISS(); waitvm<32>(); TBAR();
    issue(19u);
    TISS(); waitvm<16>(); TBAR();
    ocopy(1u, false);
    issue(20u);
    TISS(); waitvm<16>(); FSTORE(myf + 256); TBAR();
    issue(21u);
    FLOAD(fv, peerf + 256);
    issue(22u);
    TISS(); asm volatile("s_waitcnt vmcnt(16)" : "+v"(fv) :: "memory"); if (!__all(fv == tag)) GSLOW(28, gwait(peerf + 256, tag)); TBAR();
    issue(23u);
    TISS(); waitvm<33>(); TBAR();
    TISS(); waitvm<16>(); TBAR();
    TISS(); waitvm<0>(); TBAR();
    STAMP(1); STAMP_DUMP();
    return;
  }
  {
    const unsigned h_ = (unsigned)wid & 1u, c_ = ((2u * (unsigned)(gr + (bl >> 1))) & 7u) ^ ((unsigned)bl & 1u);
    if (wid < 2) {
      const rsrc_t r_ = __builtin_amdgcn_make_buffer_rsrc((void*)(X + (size_t)b * 131072 + h_ * 65536), 0, 65536, 0x00020000);
      const unsigned v0 = (lane >> 3) * 1024 + (((lane & 7) ^ (lane >> 4)) * 16), v1 = (v0 ^ 64) + 7168, v2 = v0 + 2 * 7168, v3 = v1 + 2 * 7168;
      dma4(r_, v0, v1, v2, v3, c_ * 128u, lds0 + wid * 8192); dma4(r_, v0, v1, v2, v3, c_ * 128u + 32768u, lds0 + wid * 8192 + 4096);
    } else {
      const rsrc_t r_ = __builtin_amdgcn_make_buffer_rsrc((void*)(WQ + (size_t)hp * 512 + h_ * 262144), 0, 4194304 - hp * 512 - h_ * 262144, 0x00020000);
      const unsigned v0 = ((lane >> 2) & 3) * 8192 + (lane >> 4) * 64 + (lane & 3) * 16, v1 = v0 + 31744, v2 = (v0 ^ 32) + 2 * 31744, v3 = v2 + 31744;
      dma4(r_, v0, v1, v2, v3, c_ * 524288u, lds0 + wid * 8192); dma4(r_, v0, v1, v2, v3, c_ * 524288u + 131072u, lds0 + wid * 8192 + 4096);
    }
  }
  STAMPV(35, CYC() - cyk);
  STAMP(0);
  const unsigned long long cy0 = CYC(); unsigned long long cyl = cy0; (void)cyl; unsigned wcyc = 1, wcnt = 1; (void)wcyc; (void)wcnt;
#define RNEXT(a) (((a) + PAIR) & (RING - 1))
#define LBAR() do { __builtin_amdgcn_s_waitcnt(0xC07F); BAR(); } while (0)
#define WBAR() LBAR()
#define KRD(kt, OFF, i) lds_b128((kt) + ((OFF) + 4096 * ((i) & 3)))
#define VRD(OFF, i, T) lds_tr((((i) & 1) ? ca1 : ca0) + ((OFF) + 8192 * ((i) >> 3) + 4096 * (T) + 256 * (((i) & 7) >> 1)))
#define WRD(i, T) lds_tr((((i) & 1) ? ca1 : ca0) + (8192 * ((i) >> 3) + 1024 * (T) + 256 * (((i) & 7) >> 1)))
  u32x4 qf[8], kf[16];
  s16x4 vl[38], vh[38];
  unsigned ca0, ca1, ka, kt;
  f32x4 qa[16];
  {
    const unsigned c16 = lane & 15, g4 = lane >> 4;
    const unsigned wbase = lds0 + SLOT + g4 * 2048 + ((lane >> 2) & 3) * 64 + (g4 & 1) * 32 + (lane & 3) * 8;
    const unsigned xbase = lds0 + (wid * 32 + c16) * 128 + ((g4 ^ (c16 >> 1)) * 16);
    f32x4 acc[32];
    { u32x4 z4 = {0u, 0u, 0u, 0u}; PIN(z4); NOPS(1);
#pragma unroll
      for (int d = 0; d < 32; ++d) MZ(acc[d], z4, z4); }
    u32x4 xa_[4], xb_[4];
    STAMPV(36, CYC() - cyk);
    waitvm<0>(); STAMPV(37, CYC() - cyk); BAR(); STAMPV(38, CYC() - cyk);
    ASTAMP(9);
    __builtin_amdgcn_s_waitcnt(0xC07F);
    unsigned uo = 0, xa = xbase; ca0 = wbase; ca1 = ca0 ^ 32;
    xa_[1] = lds_b128(xa + 2048); xa_[0] = lds_b128(xa);
#pragma unroll
    for (int i = 0; i < 6; ++i) { vl[i ^ 1] = WRD(i ^ 1, 0); vh[i ^ 1] = WRD(i ^ 1, 1); }
    xa_[3] = lds_b128((xa ^ 64) + 2048); xa_[2] = lds_b128(xa ^ 64);
#define QUNIT(XC, XN, FIRST, LAST)                                                                                      \
    do {                                                                                                                \
      SBAR();                                                                                                           \
      _Pragma("unroll") for (int i = 0; i < 32; ++i) {                                                                  \
        const int ai = (i >> 4) * 16 + (i & 7) * 2, xi = ((i >> 3) & 1) * 2, j = (i ^ 1) + 6, n_ = 2 * (i - 28);        \
        MA(acc[ai], CAT(vl[i], vh[i]), XC[xi]); SBAR();                                                                 \
        if (i == 9) { if (FIRST) BAR(); }                                                                               \
        if (i == 28) WBAR();                                                                                            \
        if (j < 32) vl[j] = WRD(j, 0);                                                                                  \
        else if (i >= 28 && i < 31 && !(LAST)) { vl[33 + n_] = WRD(n_ + 1, 0); vh[33 + n_] = WRD(n_ + 1, 1); }          \
        else if (i >= 28 && (LAST)) kf[(i - 28) ^ 1] = KRD(ka, 0, (i - 28) ^ 1);                                        \
        if (i == 31 && !(LAST)) { XN[3] = lds_b128((xa ^ 64) + 2048); XN[2] = lds_b128(xa ^ 64); }                      \
        if (i == 26) { if (!(LAST)) { uo = (uo == 6 * SLOT) ? 0u : uo + 3 * SLOT; xa = xbase + uo; ca0 = wbase + uo; ca1 = ca0 ^ 32; PIN(xa); PIN(ca0); PIN(ca1); } \
                       else { const unsigned l_ = lane_id(); ka = lds0 + 3 * PAIR + (l_ & 15) * 256 + (((l_ >> 4) ^ (l_ & 15)) * 16); PIN(ka); } }        \
        if ((LAST) && i >= 18 && i < 26) { const int f_ = i - 18; qf[f_] = pack2(acc[(f_ >> 1) * 4 + (f_ & 1)], acc[(f_ >> 1) * 4 + 2 + (f_ & 1)], QSCALE); PIN(qf[f_]); } \
        SBAR();                                                                                                         \
        MA(acc[ai + 1], CAT(vl[i], vh[i]), XC[xi + 1]); SBAR();                                                         \
        if (j < 32) vh[j] = WRD(j, 1);                                                                                  \
        else if (i >= 28 && i < 31 && !(LAST)) { if (i == 28) { XN[1] = lds_b128(xa + 2048); XN[0] = lds_b128(xa); } vl[32 + n_] = WRD(n_, 0); vh[32 + n_] = WRD(n_, 1); } \
        if ((LAST) && i >= 19 && i < 27) { const int f_ = i - 19; qf[f_] = natural(qf[f_]); PIN(qf[f_]); }              \
        SBAR();                                                                                                         \
      }                                                                                                                 \
      if (!(LAST)) { _Pragma("unroll") for (int i = 0; i < 6; ++i) { vl[i] = vl[32 + i]; vh[i] = vh[32 + i]; } }        \
    } while (0)
    cyl = CYC();
#pragma unroll 1
    for (int D = 0; D < 3; ++D) { QUNIT(xa_, xb_, D == 0, false); QUNIT(xb_, xa_, false, false); }
    STAMPV(32, CYC() - cyl); STAMPV(33, wcyc); cyl = CYC();
    QUNIT(xa_, xb_, false, false); QUNIT(xb_, xa_, false, true);
    STAMPV(34, CYC() - cyl);
#pragma unroll
    for (int d = 0; d < 16; ++d) qa[d] = acc[16 + d];
  }
  ASTAMP(2); STAMPV(16, CYC() - cy0); STAMPV(18, wcyc); STAMPV(19, wcnt); wcyc = 1; wcnt = 1;
  { const unsigned l_ = lane_id(); ca0 = lds0 + 3 * PAIR + (l_ >> 4) * 1024 + ((l_ >> 2) & 3) * 64 + ((l_ >> 4) & 1) * 32 + (l_ & 3) * 8; ca1 = ca0 ^ 32; }
  u32x4 ones = {0x3F803F80u, 0x3F803F80u, 0x3F803F80u, 0x3F803F80u};
  PIN(ones);
  const rsrc_t rso = __builtin_amdgcn_make_buffer_rsrc((void*)(WB + WS_O + (gj * 8) * 32768 + wid * 8192), 0, 8 * 32768 - wid * 8192, 0x00020000);
  char* const fbase = WB + WS_F + wid * 64;
  u32x4 of2[8], oa[8], ob[8], oc[8];
  f32x4 sa[8], sb[8];
#define OLD1(dst, hl, i_) dst[i_] = __builtin_amdgcn_raw_buffer_load_b128(rso, vo + ((i_) & 3) * 1024, (unsigned)(hl) * 32768u + ((i_) >> 2) * 4096, SC1)
#define ST(e) ((((e) >> 4) * 2 + (((e) >> 2) & 1)) * 2 + (((e) >> 3) & 1))
#define SV(C, e) C[ST(e)][(e) & 3]
#define EXPV(C, e) do { SV(C, e) = EXP2(SV(C, e)); PIN(C[ST(e)]); } while (0)
#define PACKP(C, q) do { pw[(q) >> 2][(q) & 3] = cvtpk(SV(C, 2 * (q)), SV(C, 2 * (q) + 1)); PIN(pw[(q) >> 2]); } while (0)
#define TILE0(PARK)                                                                                                     \
  do { const unsigned pa = lds0 + L_PARK + wid * 8192 + lane_id() * 16; u32x4 w_;                                       \
    kt = ka ^ 64; PIN(kt); NOPS(1);                                                                                     \
    SBAR();                                                                                                             \
    _Pragma("unroll") for (int i = 0; i < 16; ++i) { const int kb = i & 3, s_ = i >> 2, j = (i ^ 1) + 4;                \
      if (s_ == 0) MZ(sa[kb * 2], kf[i], qf[0]); else MA(sa[kb * 2], kf[i], qf[s_ * 2]);                                \
      SBAR();                                                                                                           \
      if ((PARK) && (i & 1) == 0 && i >= 2) *(LAS u32x4*)(size_t)(pa + ((i >> 1) - 1) * 1024) = w_;                     \
      if ((PARK) && (i & 1) == 0) { const int f_ = i >> 1; w_ = pack2(qa[(f_ >> 1) * 4 + (f_ & 1)], qa[(f_ >> 1) * 4 + 2 + (f_ & 1)], QSCALE); PIN(w_); } \
      if ((PARK) && (i & 1)) { w_ = natural(w_); PIN(w_); }                                                             \
      SBAR();                                                                                                           \
      if (s_ == 0) MZ(sa[kb * 2 + 1], kf[i], qf[1]); else MA(sa[kb * 2 + 1], kf[i], qf[s_ * 2 + 1]);                    \
      SBAR();                                                                                                           \
      if (j < 16) kf[j] = KRD(kt, 0, j); else kf[j - 16] = KRD(ka, SLOT, j - 16);                                       \
      if (i == 3 || i == 7) { kt = ka ^ (64 * ((i + 5) >> 2)); PIN(kt); }                                               \
      SBAR();                                                                                                           \
    }                                                                                                                   \
    if (PARK) *(LAS u32x4*)(size_t)(pa + 7 * 1024) = w_;                                                                \
    kt = ka ^ 64; PIN(kt);                                                                                              \
  } while (0)
#define PINO() do { _Pragma("unroll") for (int d_ = 0; d_ < 16; ++d_) PIN(o[d_]); PIN(ls[0]); PIN(ls[1]); } while (0)
#define OFINISH(OLDV)                                                                                                   \
  do { NOPS(7); PINO();                                                                                                 \
    const float inv0 = __builtin_amdgcn_rcpf(ls[0][0]), inv1 = __builtin_amdgcn_rcpf(ls[1][0]);                         \
    const unsigned pa_ = lds0 + L_PARK + wid * 8192 + lane_id() * 16;                                                   \
    _Pragma("unroll") for (int i_ = 0; i_ < 8; ++i_) OLDV[i_] = lds_b128(pa_ + i_ * 1024);                              \
    _Pragma("unroll") for (int i_ = 0; i_ < 8; ++i_) {                                                                  \
      of2[i_] = pack2(o[(i_ >> 1) * 4 + (i_ & 1)], o[(i_ >> 1) * 4 + 2 + (i_ & 1)], (i_ & 1) ? inv1 : inv0);            \
      *(LAS u32x4*)(size_t)(pa_ + i_ * 1024) = of2[i_]; }                                                               \
  } while (0)
#define TMAX(C, qb) allmax4(fmaxf(max3f(max3f(max3f(C[qb][0], C[qb][1], C[qb][2]), C[qb][3], C[2 + qb][0]), C[2 + qb][1], max3f(C[2 + qb][2], C[2 + qb][3], C[4 + qb][0])), \
                                  max3f(max3f(C[4 + qb][1], C[4 + qb][2], C[4 + qb][3]), max3f(C[6 + qb][0], C[6 + qb][1], C[6 + qb][2]), C[6 + qb][3])))
  u64 ev;
  f32x4 o[16], ls[2];
  TILE0(true);
#pragma unroll 1
  for (int hd = 0;; ++hd) {
    f32x4 negm[2];
    u32x4 pw[4];
    float fct0 = 1.f, fct1 = 1.f;
    bool pend = false;
    { u32x4 z4 = {0u, 0u, 0u, 0u}; PIN(z4); NOPS(1);
#pragma unroll
      for (int d = 0; d < 16; ++d) MZ(o[d], z4, z4);
      MZ(ls[0], z4, z4); MZ(ls[1], z4, z4);
#pragma unroll
      for (int t = 0; t < 8; ++t) PIN(sa[t]);
      SBAR();
      const float m0 = TMAX(sa, 0), m1 = TMAX(sa, 1);
#pragma unroll
      for (int t = 0; t < 8; ++t)
#pragma unroll
        for (int r = 0; r < 4; ++r) { const float v = sa[t][r] - ((t & 1) ? m1 : m0); sa[t][r] = t < 4 ? EXP2(v) : v; }
#pragma unroll
      for (int r = 0; r < 4; ++r) { negm[0][r] = -m0; negm[1][r] = -m1; }
#pragma unroll
      for (int t = 0; t < 8; ++t) PIN(sa[t]);
      PIN(negm[0]); PIN(negm[1]); NOPS(1);
    }
#define CP(i) ((i) < 4 ? 0 : (i) > 11 ? 12 : (((i) - 4) * 3 + 1) / 2)
#define STEP(C, P, LAST)                                                                                                \
    do {                                                                                                                \
      SBAR();                                                                                                           \
      _Pragma("unroll") for (int i = 0; i < 16; ++i) { const int kb = i & 3, s_ = i >> 2, j = (i ^ 1) + 4, n_ = 2 * (i - 13); \
        if (s_ == 0) MC(C[kb * 2], kf[i], qf[0], negm[0]); else MA(C[kb * 2], kf[i], qf[s_ * 2]);                       \
        SBAR();                                                                                                         \
        if (i == 13) WBAR();                                                                                            \
        if (j < 16) kf[j] = KRD(kt, SLOT, j);                                                                           \
        else if (i == 13 || i == 14) { vl[n_ + 1] = VRD(0, n_ + 1, 0); vh[n_ + 1] = VRD(0, n_ + 1, 1); }                \
        SBAR();                                                                                                         \
        if (i < 4) EXPV(P, 16 + 2 * i); else if (i < 12) EXPV(P, 20 + i);                                               \
        if (i == 12) { PACKP(P, 12); PACKP(P, 13); ca0 = RNEXT(ca0); PIN(ca0); }                                        \
        if (i == 15) { PACKP(P, 15); ka = RNEXT(ka); PIN(ka); }                                                         \
        SBAR();                                                                                                         \
        if (s_ == 0) MC(C[kb * 2 + 1], kf[i], qf[1], negm[1]); else MA(C[kb * 2 + 1], kf[i], qf[s_ * 2 + 1]);           \
        SBAR();                                                                                                         \
        if (i == 13 || i == 14) { vl[n_] = VRD(0, n_, 0); vh[n_] = VRD(0, n_, 1); }                                     \
        SBAR();                                                                                                         \
        if (i < 4) EXPV(P, 17 + 2 * i);                                                                                 \
        _Pragma("unroll") for (int q = CP(i); q < CP(i + 1); ++q) PACKP(P, q);                                          \
        if (i == 12) { PACKP(P, 14); ca1 = ca0 ^ 32; PIN(ca1); }                                                        \
        if (i == 3 || i == 7) { kt = ka ^ (64 * ((i + 5) >> 2)); PIN(kt); }                                             \
        SBAR();                                                                                                         \
      }                                                                                                                 \
      float m0 = 0.f, m1 = 0.f;                                                                                         \
      _Pragma("unroll") for (int i = 0; i < 16; ++i) { const int e_ = i & 7, u_ = i >> 3, j = (i ^ 1) + 4, n_ = 2 * (i - 13); \
        MA(o[e_ * 2], CAT(vl[i], vh[i]), pw[u_ * 2]);                                                                   \
        SBAR();                                                                                                         \
        if ((LAST) && i == 13) WBAR();                                                                                  \
        if (j < 16) vl[j] = VRD(0, j, 0);                                                                               \
        else if (!(LAST)) kf[j - 16] = KRD(ka, SLOT, j - 16);                                                           \
        else if (i == 13 || i == 14) { vl[17 + n_] = VRD(SLOT, n_ + 1, 0); vh[17 + n_] = VRD(SLOT, n_ + 1, 1); }        \
        SBAR();                                                                                                         \
        if (i == 1) { m0 = max3f(C[0][0], C[0][1], C[0][2]); m1 = max3f(C[0][3], C[1][0], C[1][1]); PIN(m0); PIN(m1); } \
        if (i >= 2 && i <= 4) { m0 = max3f(m0, C[2 * i - 2][2], C[2 * i - 2][3]); m1 = max3f(m1, C[2 * i - 1][0], C[2 * i - 1][1]); PIN(m0); PIN(m1); } \
        if (i >= 6 && i < 14) EXPV(C, 2 * (i - 6));                                                                     \
        SBAR();                                                                                                         \
        MA(o[e_ * 2 + 1], CAT(vl[i], vh[i]), pw[u_ * 2 + 1]);                                                           \
        SBAR();                                                                                                         \
        if (j < 16) vh[j] = VRD(0, j, 1);                                                                               \
        else if ((LAST) && (i == 13 || i == 14)) { vl[16 + n_] = VRD(SLOT, n_, 0); vh[16 + n_] = VRD(SLOT, n_, 1); }    \
        SBAR();                                                                                                         \
        if (i >= 1 && i <= 3) { m0 = max3f(m0, C[2 * i - 1][2], C[2 * i - 1][3]); m1 = max3f(m1, C[2 * i][0], C[2 * i][1]); PIN(m0); PIN(m1); } \
        if (i == 4) { m0 = max3f(m0, C[7][2], C[7][3]); PIN(m0); }                                                      \
        if (i == 5) {                                                                                                   \
          if (__builtin_expect(__any((m0 > THR) | (m1 > THR)), 0)) {                                                    \
            const float d0 = fmaxf(TMAX(C, 0), 0.f), d1 = fmaxf(TMAX(C, 1), 0.f), a1 = lane_id() < 16u ? 1.f : 0.f;     \
            fct0 = EXP2(-d0); fct1 = EXP2(-d1); pend = true;                                                            \
            add_col5(C[0], C[2], C[4], C[6], negm[0], a1, -d0); add_col5(C[1], C[3], C[5], C[7], negm[1], a1, -d1);     \
          } }                                                                                                           \
        if (i >= 6 && i < 14) EXPV(C, 2 * (i - 6) + 1);                                                                 \
        if (i == 15 && !(LAST)) { kt = ka ^ 64; PIN(kt); }                                                              \
        SBAR();                                                                                                         \
        if ((i & 3) == 3) { MA(ls[(i >> 2) & 1], ones, pw[i >> 2]); SBAR(); }                                           \
      }                                                                                                                 \
      if (__builtin_expect(pend, 0)) { NOPS(7); PINO();                                                                       \
        _Pragma("unroll") for (int d = 0; d < 16; ++d) _Pragma("unroll") for (int e = 0; e < 4; ++e) o[d][e] *= (d & 1) ? fct1 : fct0;  \
        ls[0][0] *= fct0; ls[1][0] *= fct1; pend = false;                                                               \
        PINO(); NOPS(1); }                                                                              \
    } while (0)
#pragma unroll 1
    for (int t = 1; t < 7; t += 2) {
      STEP(sb, sa, false);
      STEP(sa, sb, false);
    }
    STEP(sb, sa, true);
    {
      ev = gld(WB + WS_E);
#pragma unroll
      for (int q = 0; q < 8; ++q) PACKP(sb, q);
      NOPS(1);
      SBAR();
#pragma unroll
      for (int i = 0; i < 16; ++i) { const int e_ = i & 7, u_ = i >> 3, j = (i ^ 1) + 4;
        MA(o[e_ * 2], CAT(vl[16 + i], vh[16 + i]), pw[u_ * 2]);
        SBAR();
        if (j < 16) vl[16 + j] = VRD(SLOT, j, 0);
        SBAR();
        if (i < 6) EXPV(sb, 16 + 3 * i);
        if (i < 5) EXPV(sb, 17 + 3 * i);
        if (i == 6) { PACKP(sb, 12); PACKP(sb, 13); }
        if (i == 7) { PACKP(sb, 15); }
        SBAR();
        MA(o[e_ * 2 + 1], CAT(vl[16 + i], vh[16 + i]), pw[u_ * 2 + 1]);
        SBAR();
        if (j < 16) vh[16 + j] = VRD(SLOT, j, 1);
        SBAR();
        if (i < 5) EXPV(sb, 18 + 3 * i);
        if (i >= 2 && i < 6) PACKP(sb, 6 + i);
        if (i == 6) PACKP(sb, 14);
        SBAR();
        if (i == 1 || i == 3 || i == 9 || i == 11) { MA(ls[(i >> 1) & 1], ones, pw[(i >> 3) * 2 + ((i >> 1) & 1)]); SBAR(); }
      }
      if (hd == 1) break;
      OFINISH(qf);
      WBAR();
      ka = RNEXT(ka); ca0 = RNEXT(ca0); ca1 = ca0 ^ 32;
      kf[1] = KRD(ka, 0, 1); kf[0] = KRD(ka, 0, 0); kf[3] = KRD(ka, 0, 3); kf[2] = KRD(ka, 0, 2);
      ASTAMP(3);
      TILE0(false);
    }
  }
  OFINISH(oa);
  u64 tag = (((u64)(unsigned)__builtin_amdgcn_readfirstlane((unsigned)(ev >> 32)) << 32) | (unsigned)__builtin_amdgcn_readfirstlane((unsigned)ev)) + 1ull;
  asm volatile("" : "+s"(tag));
  ASTAMP(4); STAMPV(17, CYC() - cy0); STAMPV(20, wcyc); STAMPV(21, wcnt); wcyc = 1; cyl = CYC();
  {
    const unsigned l_ = lane_id(), vo = l_ * 16;
    f32x4 acc[16];
#define FIDX(NC, i) ((NC) == 8 ? (i) : ((i) / 6) * 8 + 2 + (i) % 6)
#define OPAIR(B, FIRST, NOBAR, NC, NN, MID)                                                                                    \
    do {                                                                                                                \
      SBAR();                                                                                                           \
      _Pragma("unroll") for (int i = 0; i < 4 * (NC); ++i) {                                                            \
        const int f_ = FIDX(NC, i), N_ = 4 * (NC), j = (i ^ 1) + 6, n_ = 2 * (i - (N_ - 4));                            \
        if ((FIRST) && i < 8) MZ(acc[(f_ & 7) * 2], CAT(vl[i], vh[i]), B[0]); else MA(acc[(f_ & 7) * 2], CAT(vl[i], vh[i]), B[(f_ >> 3) * 2]); \
        SBAR();                                                                                                         \
        if ((NN) && i == N_ - 4 && !(NOBAR)) WBAR();                                                                    \
        if (j < N_) vl[j] = VRD(0, FIDX(NC, j), 0);                                                                     \
        else if ((NN) && i >= N_ - 4 && i < N_ - 1) { vl[N_ + n_ + 1] = VRD(0, FIDX(NN, n_ + 1), 0); vh[N_ + n_ + 1] = VRD(0, FIDX(NN, n_ + 1), 1); } \
        if ((NN) && i == N_ - 6) { ca0 = RNEXT(ca0); ca1 = ca0 ^ 32; PIN(ca0); PIN(ca1); }                              \
        { MID; }                                                                                                        \
        SBAR();                                                                                                         \
        if ((FIRST) && i < 8) MZ(acc[(f_ & 7) * 2 + 1], CAT(vl[i], vh[i]), B[1]); else MA(acc[(f_ & 7) * 2 + 1], CAT(vl[i], vh[i]), B[(f_ >> 3) * 2 + 1]); \
        SBAR();                                                                                                         \
        if (j < N_) vh[j] = VRD(0, FIDX(NC, j), 1);                                                                     \
        else if ((NN) && i >= N_ - 4 && i < N_ - 1) { vl[N_ + n_] = VRD(0, FIDX(NN, n_), 0); vh[N_ + n_] = VRD(0, FIDX(NN, n_), 1); } \
        SBAR();                                                                                                         \
      }                                                                                                                 \
      if (NN) { _Pragma("unroll") for (int i = 0; i < 6; ++i) { vl[i] = vl[4 * (NC) + i]; vh[i] = vh[4 * (NC) + i]; } } \
    } while (0)
    WBAR();
    ca0 = RNEXT(ca0); ca1 = ca0 ^ 32;
#pragma unroll
    for (int i = 0; i < 6; ++i) { vl[i ^ 1] = VRD(0, i ^ 1, 0); vh[i ^ 1] = VRD(0, i ^ 1, 1); }
#define OFET(dst, hl, i0, st) if (i >= (i0) && (i - (i0)) % (st) == 0 && (i - (i0)) / (st) < 8) OLD1(dst, hl, ((i - (i0)) / (st)) & 7)
    OPAIR(of2, true, false, 8, 8, { OFET(ob, 2 * ((gr + 1) & 3), 0, 2); OFET(oc, 2 * ((gr + 2) & 3), 13, 2); });
    OPAIR(oa, false, true, 8, 8, {});
    OPAIR(ob, false, false, 8, 8, { OFET(of2, 2 * ((gr + 3) & 3), 12, 2); });
    ASTAMP(5);
    OPAIR(oc, false, false, 8, 8, { OFET(oa, 2 * ((gr + 1) & 3) + 1, 28, 1); });
    ASTAMP(6);
    OPAIR(of2, false, false, 8, 6, { if (i < 4) OLD1(oa, 2 * ((gr + 1) & 3) + 1, (4 + i) & 7); OFET(ob, 2 * ((gr + 2) & 3) + 1, 6, 2); OFET(oc, 2 * ((gr + 3) & 3) + 1, 15, 2); });
    OPAIR(oa, false, false, 6, 6, {});
    OPAIR(ob, false, false, 6, 6, {});
    OPAIR(oc, false, false, 6, 0, {});
    ASTAMP(7); STAMPV(25, CYC() - cyl);
    const rsrc_t rp = __builtin_amdgcn_make_buffer_rsrc((void*)(WB + WS_P + gr * 131072 + wid * 2048), 0, 131072 - wid * 2048, 0x00020000);
    { unsigned c4[3];
      c4[2] = ca0; c4[0] = RNEXT(RNEXT(ca0)); c4[1] = RNEXT(c4[0]);
#define DRD(i, T) lds_tr((c4[(i) >> 3] ^ (((i) & 1) * 32)) + (8192 * (((i) >> 1) & 3) + 4096 * (T)))
#pragma unroll
      for (int i = 0; i < 6; ++i) { vl[i ^ 1] = DRD(i ^ 1, 0); vh[i ^ 1] = DRD(i ^ 1, 1); }
      SBAR();
#pragma unroll
      for (int i = 0; i < 6; ++i) { const int s = 1 + (i >> 1);
        const unsigned va = vo + ((((unsigned)(gj + s) & 3u) * 4u + (unsigned)gj) * 8192u);
        __builtin_amdgcn_raw_buffer_store_b128(pack2n(acc[4 * s + (i & 1)], acc[4 * s + 2 + (i & 1)]), rp, va + (i & 1) * 1024, 0, SC1); }
      SBAR();
#pragma unroll
      for (int i = 0; i < 24; ++i) { const int j = (i ^ 1) + 6, k_ = ((i >> 1) & 3) * 2;
        const u32x4 b0 = (i >> 3) == 0 ? oa[k_] : (i >> 3) == 1 ? ob[k_] : oc[k_];
        const u32x4 b1 = (i >> 3) == 0 ? oa[k_ + 1] : (i >> 3) == 1 ? ob[k_ + 1] : oc[k_ + 1];
        MA(acc[(i & 1) * 2], CAT(vl[i], vh[i]), b0);
        SBAR();
        if (j < 24) vl[j] = DRD(j, 0);
        SBAR();
        MA(acc[(i & 1) * 2 + 1], CAT(vl[i], vh[i]), b1);
        SBAR();
        if (j < 24) vh[j] = DRD(j, 1);
        SBAR();
      }
      GSLOW(31, waitvm<0>()); if (l_ == 0) gst(fbase + hp * 768 + 512, tag);
    }
    ASTAMP(10);
    GSLOW(29, gwait(fbase + (4 * ((gj + 1 + (l_ & 3) % 3) & 3) + gr) * 768 + 512, tag));
    ASTAMP(13);
    u32x4 pp[3][2];
#pragma unroll
    for (int s = 0; s < 3; ++s) {
      const unsigned src = (unsigned)(gj + 1 + s) & 3u;
      pp[s][0] = __builtin_amdgcn_raw_buffer_load_b128(rp, vo, ((unsigned)gj * 4 + src) * 8192u, SC1);
      pp[s][1] = __builtin_amdgcn_raw_buffer_load_b128(rp, vo + 1024, ((unsigned)gj * 4 + src) * 8192u, SC1);
    }
    if (wid == 0 && hp == 0 && l_ == 0) gst(WB + WS_E, tag);
#pragma unroll
    for (int qb = 0; qb < 2; ++qb) {
#pragma unroll
      for (int s = 0; s < 3; ++s)
#pragma unroll
        for (int w = 0; w < 4; ++w) {
          const unsigned x = pp[s][qb][w];
          acc[(w >> 1) * 2 + qb][(w & 1) * 2] += __uint_as_float(x << 16); acc[(w >> 1) * 2 + qb][(w & 1) * 2 + 1] += __uint_as_float(x & 0xffff0000u);
        }
      *(GAS u32x4*)(OUT + ((size_t)(b * 128 + wid * 32 + qb * 16 + (l_ & 15)) * 512 + gr * 128 + gj * 32) * 2 + (l_ >> 4) * 16) = natural(pack2n(acc[qb], acc[2 + qb]));
    }
  }
  STAMPV(59, CYC() - cy0); STAMP(14); STAMPV(23, wcyc); STAMPV(26, 1 + (__builtin_amdgcn_s_getreg((20 << 0) | (0 << 6) | (3 << 11)) & 15)); STAMP_DUMP();
}

__global__ void t_first(unsigned* p) { if (threadIdx.x == 0) __hip_atomic_store((GAS unsigned*)p, (unsigned)__builtin_amdgcn_s_memrealtime(), __ATOMIC_RELAXED, __HIP_MEMORY_SCOPE_AGENT); }
__global__ void t_last(unsigned* p) { const unsigned t0 = __hip_atomic_load((GAS unsigned*)p, __ATOMIC_RELAXED, __HIP_MEMORY_SCOPE_AGENT);
  for (int i = 0; i < 100000 && (unsigned)__builtin_amdgcn_s_memrealtime() - t0 < 10000u; ++i) __builtin_amdgcn_s_sleep(1); }
extern "C" void kernel_launch(void* const* d_in, const int* in_sizes, int n_in, void* d_out, int out_size, void* d_ws, size_t ws_size, hipStream_t stream) {
  const char* x = (const char*)d_in[0]; const char* k = (const char*)d_in[1]; const char* v = (const char*)d_in[2];
  const char* wq = (const char*)d_in[3]; const char* wo = (const char*)d_in[4];
  (void)hipFuncSetAttribute((const void*)mha_fused, hipFuncAttributeMaxDynamicSharedMemorySize, (int)L_END);
  t_first<<<1, 64, 0, stream>>>((unsigned*)((char*)d_ws + (100u << 20)));
  mha_fused<<<256, 384, L_END, stream>>>(x, k, v, wq, wo, (char*)d_ws, (char*)d_out);
  t_last<<<1, 64, 0, stream>>>((unsigned*)((char*)d_ws + (100u << 20)));
}
```

```cpp
#include <hip/hip_runtime.h>
#include <hip/hip_bf16.h>

typedef short    s16x4  __attribute__((ext_vector_type(4)));
typedef float    f32x4  __attribute__((ext_vector_type(4)));
typedef float    f32x2  __attribute__((ext_vector_type(2)));
typedef __bf16   bf16x2 __attribute__((ext_vector_type(2)));
typedef unsigned u32x4  __attribute__((ext_vector_type(4)));
typedef unsigned long long u64;
typedef __amdgpu_buffer_rsrc_t rsrc_t;
#define LAS __attribute__((address_space(3)))
#define GAS __attribute__((address_space(1)))
#define DI __device__ __forceinline__
#define MA(D, A, B) asm volatile("v_mfma_f32_16x16x32_bf16 %0, %1, %2, %0" : "+v"(D) : "v"(A), "v"(B))
#define MC(D, A, B, C) asm volatile("v_mfma_f32_16x16x32_bf16 %0, %1, %2, %3" : "=&v"(D) : "v"(A), "v"(B), "v"(C))
#define MZ(D, A, B) asm volatile("v_mfma_f32_16x16x32_bf16 %0, %1, %2, 0" : "=&v"(D) : "v"(A), "v"(B))
#define NOPS(n) asm volatile("s_nop " #n)

constexpr float QSCALE = 0.08838834764831845f * 1.4426950408889634f;
constexpr float THR = 8.0f;

constexpr unsigned SLOT = 16384, PAIR = 32768, RING = 4 * PAIR;
constexpr unsigned L_PARK = RING, L_END = L_PARK + 32768;
constexpr size_t WS_B = 2u << 20;
constexpr size_t WS_O = 4096;
constexpr size_t WS_P = WS_O + (1u << 20);
constexpr size_t WS_F = WS_P + (512u << 10);
constexpr size_t WS_E = WS_F + 16384;
constexpr size_t WS_T = WS_E + 4096;

DI unsigned cvtpk(float lo, float hi) { f32x2 v = {lo, hi}; bf16x2 b = __builtin_convertvector(v, bf16x2); return __builtin_bit_cast(unsigned, b); }
DI u32x4 pack2n(const f32x4& t0, const f32x4& t1) { return u32x4{cvtpk(t0[0], t0[1]), cvtpk(t0[2], t0[3]), cvtpk(t1[0], t1[1]), cvtpk(t1[2], t1[3])}; }
#define SPACK(d0, d1, t, sc) do { unsigned x_, y_; asm volatile("v_mul_f32_e32 %0, %4, %5\n\tv_mul_f32_e32 %2, %4, %6\n\tv_mul_f32_e32 %1, %4, %7\n\tv_mul_f32_e32 %3, %4, %8\n\t" \
    "v_cvt_pk_bf16_f32 %0, %0, %2\n\tv_cvt_pk_bf16_f32 %1, %1, %3" : "=&v"(d0), "=&v"(d1), "=&v"(x_), "=&v"(y_) : "v"(sc), "v"((t)[0]), "v"((t)[1]), "v"((t)[2]), "v"((t)[3])); } while (0)
DI u32x4 natural(u32x4 w) {
  auto a0 = __builtin_amdgcn_permlane32_swap(w[0], w[2], false, false); auto a1 = __builtin_amdgcn_permlane32_swap(w[1], w[3], false, false);
  auto b0 = __builtin_amdgcn_permlane16_swap(a0[0], a0[1], false, false); auto b1 = __builtin_amdgcn_permlane16_swap(a1[0], a1[1], false, false);
  return u32x4{b0[0], b1[0], b0[1], b1[1]};
}
DI float allmax4(float m) {
  auto r = __builtin_amdgcn_permlane16_swap(__float_as_uint(m), __float_as_uint(m), false, false); m = fmaxf(__uint_as_float(r[0]), __uint_as_float(r[1]));
  auto q = __builtin_amdgcn_permlane32_swap(__float_as_uint(m), __float_as_uint(m), false, false); return fmaxf(__uint_as_float(q[0]), __uint_as_float(q[1]));
}
DI u32x4 lds_b128(unsigned a) { return *(const LAS u32x4*)(size_t)a; }
DI s16x4 lds_tr(unsigned a) { return __builtin_amdgcn_ds_read_tr16_b64_v4i16((LAS s16x4*)(size_t)a); }
#define CAT(lo, hi) __builtin_shufflevector(lo, hi, 0, 1, 2, 3, 4, 5, 6, 7)

template <int N> DI void waitvm() { asm volatile("s_waitcnt vmcnt(%0)" ::"i"(N) : "memory"); }
DI void dma4_nt(rsrc_t r, unsigned v0, unsigned v1, unsigned v2, unsigned v3, unsigned soff, unsigned m0v) {
  asm volatile("s_mov_b32 m0, %0\n\ts_nop 0\n\t"
               "buffer_load_dwordx4 %1, %5, %6 offen nt lds\n\t"
               "buffer_load_dwordx4 %2, %5, %6 offen offset:1024 nt lds\n\t"
               "buffer_load_dwordx4 %3, %5, %6 offen offset:2048 nt lds\n\t"
               "buffer_load_dwordx4 %4, %5, %6 offen offset:3072 nt lds" ::"s"(m0v), "v"(v0), "v"(v1), "v"(v2), "v"(v3), "s"(r), "s"(soff) : "memory");
}
DI void dma4(rsrc_t r, unsigned v0, unsigned v1, unsigned v2, unsigned v3, unsigned soff, unsigned m0v) {
  asm volatile("s_mov_b32 m0, %0\n\ts_nop 0\n\t"
               "buffer_load_dwordx4 %1, %5, %6 offen lds\n\t"
               "buffer_load_dwordx4 %2, %5, %6 offen offset:1024 lds\n\t"
               "buffer_load_dwordx4 %3, %5, %6 offen offset:2048 lds\n\t"
               "buffer_load_dwordx4 %4, %5, %6 offen offset:3072 lds" ::"s"(m0v), "v"(v0), "v"(v1), "v"(v2), "v"(v3), "s"(r), "s"(soff) : "memory");
}
constexpr unsigned GSPIN_MAX = 1u << 18;
#define BAR() asm volatile("s_barrier" ::: "memory")
#define SBAR() __builtin_amdgcn_sched_barrier(0)
#define PIN(x) asm volatile("" : "+v"(x))
DI float max3f(float a, float b, float c) { return __builtin_fmaxf(__builtin_fmaxf(a, b), c); }
DI void add_col5(f32x4& c0, f32x4& c1, f32x4& c2, f32x4& c3, f32x4& c4, float aone, float v) {
  asm volatile("s_nop 1\n\tv_mfma_f32_16x16x4_f32 %0, %5, %6, %0\n\tv_mfma_f32_16x16x4_f32 %1, %5, %6, %1\n\tv_mfma_f32_16x16x4_f32 %2, %5, %6, %2\n\t"
               "v_mfma_f32_16x16x4_f32 %3, %5, %6, %3\n\tv_mfma_f32_16x16x4_f32 %4, %5, %6, %4\n\ts_nop 15"
               : "+v"(c0), "+v"(c1), "+v"(c2), "+v"(c3), "+v"(c4) : "v"(aone), "v"(v));
}
DI unsigned lane_id() { unsigned l; asm volatile("v_mbcnt_lo_u32_b32 %0, -1, 0\n\tv_mbcnt_hi_u32_b32 %0, -1, %0" : "=v"(l)); return l; }
#define EXP2(x) __builtin_amdgcn_exp2f(x)
DI u64 next_tag(u64 e) { return e * 6364136223846793005ull + 1442695040888963407ull; }
DI u64 gld(const char* p) { return __hip_atomic_load((const GAS u64*)p, __ATOMIC_RELAXED, __HIP_MEMORY_SCOPE_AGENT); }
DI void gst(char* p, u64 v) { __hip_atomic_store((GAS u64*)p, v, __ATOMIC_RELAXED, __HIP_MEMORY_SCOPE_AGENT); }
DI void gwait(const char* p, u64 tag) {
#pragma unroll 1
  for (unsigned s = 0; s < GSPIN_MAX; ++s) { if (__all(gld(p) == tag)) break; }
  asm volatile("" ::: "memory");
}
constexpr int SC1 = 16;
constexpr unsigned CODE_BYTES = 45696;

__global__ __launch_bounds__(384, 2) void mha_fused(const char* X, const char* K, const char* V,
                                                    const char* WQ, const char* WO, char* WS, char* OUT) {
  extern __shared__ __attribute__((aligned(16))) char shm[];
  const unsigned lds0 = (unsigned)(size_t)(LAS char*)shm;
  asm volatile("" : "+s"(X), "+s"(K), "+s"(V), "+s"(WQ), "+s"(WO), "+s"(WS), "+s"(OUT));
  const int tid = threadIdx.x, lane = tid & 63;
  const int wid = __builtin_amdgcn_readfirstlane(tid >> 6);
  const int g = blockIdx.x & 7, idx = blockIdx.x >> 3;
  const int bl = idx & 7, b = 8 * (g & 1) + bl, gj = g >> 1, gr = idx >> 3, hp = 4 * gj + gr;
  const unsigned rot = 4u * ((unsigned)(bl ^ gr) & 1u);
  char* const WB = WS + (size_t)b * WS_B;
  u64 pc0; asm volatile("s_getpc_b64 %0" : "=s"(pc0));
  if (wid >= 4) {
    const unsigned L = (unsigned)wid - 4u;
    __builtin_amdgcn_s_setprio(3);

    const rsrc_t rx = __builtin_amdgcn_make_buffer_rsrc((void*)(X + (size_t)b * 131072 + L * 65536), 0, 131072 - L * 65536, 0x00020000);
    const rsrc_t rw = __builtin_amdgcn_make_buffer_rsrc((void*)(WQ + (size_t)hp * 512 + L * 262144), 0, 4194304 - hp * 512 - L * 262144, 0x00020000);
    const unsigned t4 = (lane >> 4) * 64 + (lane & 3) * 16, r4 = (lane >> 2) & 3;
    unsigned vx[4], vw[4];
#pragma unroll
    for (int j = 0; j < 4; ++j) {
      vx[j] = (lane >> 3) * 1024 + (((lane & 7) ^ ((4 * j + (lane >> 4)) & 7)) * 16) + j * (8192 - 1024);
      vw[j] = r4 * 8192 + (t4 ^ ((j >> 1) * 32)) + j * (32768 - 1024);
    }
    const unsigned m0b = lds0 + L * 8192;
#define HALF_X(so, m) do { dma4(rx, vx[0], vx[1], vx[2], vx[3], (so), (m)); dma4(rx, vx[0], vx[1], vx[2], vx[3], (so) + 32768u, (m) + 4096u); } while (0)
#define HALF_W(so, m) do { dma4(rw, vw[0], vw[1], vw[2], vw[3], (so), (m)); dma4(rw, vw[0], vw[1], vw[2], vw[3], (so) + 131072u, (m) + 4096u); } while (0)
#define HALF_O(so, m) do { waitvm<52>(); dma4(ro, vo[0], vo[1], vo[2], vo[3], (so), (m)); dma4(ro, vo[0], vo[1], vo[2], vo[3], (so) + 16384u, (m) + 4096u); } while (0)
#define HALF_K(so, m) do { waitvm<12>(); dma4_nt(rk, vk[0], vk[1], vk[2], vk[3], (so), (m)); waitvm<12>(); dma4_nt(rk, vk[0], vk[1], vk[2], vk[3], (so) + 4096u, (m) + 4096u); } while (0)
#define HALF_V(so, m) do { waitvm<12>(); dma4_nt(rv, vv, vu, vv, vu, (so), (m)); waitvm<12>(); dma4_nt(rv, vv, vu, vv, vu, (so) + 4096u, (m) + 4096u); } while (0)
#define UNIT(u, r) do { waitvm<38>(); const unsigned c_ = ((u) + rot) & 7u, m_ = m0b + (r) * 3 * SLOT; HALF_X(c_ * 128u, m_); HALF_W(c_ * 524288u, m_ + SLOT); HALF_W(c_ * 524288u + 256u, m_ + 2 * SLOT); } while (0)
#define FLOAD(dst, p) asm volatile("global_load_dwordx2 %0, %1, off sc1" : "=v"(dst) : "v"(p) : "memory")
    HALF_W(rot * 524288u + 256u, m0b + 2 * SLOT);
    u64 fv;
    HALF_X((rot + 1u) * 128u, m0b + 3 * SLOT);
    const size_t kvb = ((size_t)b * 16 + hp) * 262144 + L * 8192;
    const rsrc_t rk = __builtin_amdgcn_make_buffer_rsrc((void*)(K + kvb), 0, 262144 - L * 8192, 0x00020000);
    const rsrc_t rv = __builtin_amdgcn_make_buffer_rsrc((void*)(V + kvb), 0, 262144 - L * 8192, 0x00020000);
    const rsrc_t ro = __builtin_amdgcn_make_buffer_rsrc((void*)(WO + (size_t)gj * 1048576 + gr * 256 + L * 32768), 0, 1048576 - gr * 256 - L * 32768, 0x00020000);
    unsigned vk[4], vo[4];
#pragma unroll
    for (int j = 0; j < 4; ++j) {
      vk[j] = (lane >> 4) * 256 + (((lane & 15) ^ (4 * j + (lane >> 4))) * 16);
      vo[j] = r4 * 1024 + (((lane >> 4) + gj) & 3) * 64 + (((lane & 3) * 16) ^ ((j & 1) * 32)) + j * (4096 - 1024);
    }
    const unsigned vv = r4 * 256 + t4, vu = vv ^ 32;
    const char* const peerf = WB + WS_F + (4 * gj + ((gr + 1 + (lane & 3) % 3) & 3)) * 768 + ((lane >> 2) & 1) * 64;
    char* const myf = WB + WS_F + hp * 768 + L * 64;
    const rsrc_t rs = __builtin_amdgcn_make_buffer_rsrc((void*)(WB + WS_O + (size_t)(2 * hp) * 32768 + L * 16384), 0, 65536 - L * 16384, 0x00020000);
    auto ocopy = [&](unsigned hd) {
      u32x4 t[16];
#pragma unroll
      for (int i = 0; i < 16; ++i) t[i] = *(const LAS u32x4*)(size_t)(lds0 + L_PARK + L * 16384 + lane * 16 + i * 1024);
#pragma unroll
      for (int i = 0; i < 16; ++i) __builtin_amdgcn_raw_buffer_store_b128(t[i], rs, lane * 16 + hd * 32768u + i * 1024, 0, SC1);
      asm volatile("" ::: "memory");
    };
#define FSTORE(p) do { if (lane == 0) gst((p), tag); } while (0)
    auto issue = [&](unsigned q) {
      const unsigned m = m0b + ((q + 3u) & 3u) * PAIR;
      if (q < 16) {
        const unsigned p = q & 7u, hb = (q & 8u) * SLOT;
        if (p == 0) HALF_K(hb, m); else HALF_V(hb + (p - 1u) * SLOT, m);
        if (p < 7) HALF_K(hb + (p + 1u) * SLOT, m + SLOT); else HALF_V(hb + 7u * SLOT, m + SLOT);
      } else {
        const unsigned s = q - 16u, hl = s == 0 ? 2u * gr + 1u : (s < 5 ? 2u * ((gr + s - 1u) & 3u) : 2u * ((gr + s - 4u) & 3u) + 1u);
        HALF_O(hl * 131072u, m); HALF_O(hl * 131072u + 65536u, m + SLOT);
      }
    };

    u64 tag;
    asm volatile("s_load_dwordx2 %0, %1, 0x0" : "=s"(tag) : "s"(WB + WS_E) : "memory");
    unsigned junk = 0;
#define TOUCH2(p, q) asm volatile("s_load_dword %0, %1, 0x0\n\ts_load_dword %0, %2, 0x0" : "+s"(junk) : "s"(p), "s"(q) : "memory")
    { const size_t o_ = ((size_t)b * 16 + hp) * 262144 + L * 131072;
      TOUCH2(K + o_, V + o_);
      TOUCH2(WO + (size_t)gj * 1048576 + L * 1048572, (const char*)WB + L * WS_T);
      TOUCH2(OUT + (size_t)b * 131072 + L * 65536, OUT + (size_t)b * 131072 + L * 65536 + 65532); }

    BAR();
    HALF_W((rot + 1u) * 524288u, m0b + 4 * SLOT); HALF_W((rot + 1u) * 524288u + 256u, m0b + 5 * SLOT);
    waitvm<24>(); BAR();
    u32x4 keep = {0u, 0u, 0u, 0u};
#define WOPREF() do { _Pragma("unroll") for (int i = 0; i < 4; ++i)                                                     \
        asm volatile("buffer_load_dwordx4 %0, %1, %5, %6 offen\n\tbuffer_load_dwordx4 %0, %2, %5, %6 offen offset:1024\n\t" \
                     "buffer_load_dwordx4 %0, %3, %5, %6 offen offset:2048\n\tbuffer_load_dwordx4 %0, %4, %5, %6 offen offset:3072" \
                     : "+v"(keep) : "v"(vo[0]), "v"(vo[1]), "v"(vo[2]), "v"(vo[3]), "s"(ro), "s"((unsigned)bl * 131072u + (i >> 1) * 65536u + (i & 1) * 16384u) : "memory"); } while (0)
    UNIT(2u, 2);
    waitvm<24>(); __builtin_amdgcn_s_sleep(10); BAR();
    UNIT(3u, 0);
    waitvm<24>(); __builtin_amdgcn_s_sleep(10); BAR();
    UNIT(4u, 1);
    waitvm<24>(); BAR();
    UNIT(5u, 2);
    waitvm<24>(); BAR();
    UNIT(6u, 0);
    waitvm<44>(); WOPREF();
    waitvm<40>(); BAR();
    UNIT(7u, 1);
    asm volatile("s_waitcnt lgkmcnt(0)" : "+s"(junk), "+s"(tag) :: "memory"); tag = next_tag(tag);
    waitvm<40>(); BAR();
    issue(0u);
    asm volatile("s_waitcnt vmcnt(16)" : "+v"(keep) :: "memory"); BAR();
    issue(1u);
    waitvm<16>(); BAR();
    issue(2u);
    issue(3u);
    waitvm<32>(); BAR();
    issue(4u);
    waitvm<32>(); BAR();
    issue(5u);
    waitvm<32>(); BAR();
    issue(6u);
    waitvm<32>(); BAR();
    issue(7u);
    waitvm<32>(); BAR();
    issue(8u);
    waitvm<32>(); BAR();
    issue(9u);
    waitvm<40>(); BAR();
    waitvm<32>(); BAR();
    issue(10u);
    waitvm<32>(); BAR();
    issue(11u);
    waitvm<32>(); BAR();
    waitvm<44>(); ocopy(0u);
    issue(12u);
    waitvm<48>(); BAR();
    waitvm<32>(); BAR();
    issue(13u);
    waitvm<16>(); FSTORE(myf); BAR();
    issue(14u);
    waitvm<17>(); BAR();
    issue(15u);
    waitvm<16>(); BAR();
    FLOAD(fv, peerf);
    issue(16u);
    waitvm<25>(); BAR();
    issue(17u);
    issue(18u);
    asm volatile("s_waitcnt vmcnt(48)" : "+v"(fv) :: "memory"); if (!__all(fv == tag)) gwait(peerf, tag); BAR();

    waitvm<16>(); BAR();
    ocopy(1u);
    issue(19u);
    waitvm<16>(); FSTORE(myf + 256); BAR();
    issue(20u);
    FLOAD(fv, peerf + 256);
    asm volatile("s_waitcnt vmcnt(0)" : "+v"(fv) :: "memory"); if (!__all(fv == tag)) gwait(peerf + 256, tag); BAR();
    issue(21u);
    waitvm<17>(); BAR();
    issue(22u);
    waitvm<16>(); BAR();
    issue(23u);
    waitvm<16>(); BAR();
    waitvm<0>(); BAR();
    return;
  }
  {
    const unsigned h_ = (unsigned)wid & 1u, c_ = rot;
    if (wid < 2) {
      const rsrc_t r_ = __builtin_amdgcn_make_buffer_rsrc((void*)(X + (size_t)b * 131072 + h_ * 65536), 0, 65536, 0x00020000);
      const unsigned v0 = (lane >> 3) * 1024 + (((lane & 7) ^ (lane >> 4)) * 16), v1 = (v0 ^ 64) + 7168, v2 = v0 + 2 * 7168, v3 = v1 + 2 * 7168;
      dma4(r_, v0, v1, v2, v3, c_ * 128u, lds0 + wid * 8192); dma4(r_, v0, v1, v2, v3, c_ * 128u + 32768u, lds0 + wid * 8192 + 4096);
    } else {
      const rsrc_t r_ = __builtin_amdgcn_make_buffer_rsrc((void*)(WQ + (size_t)hp * 512 + h_ * 262144), 0, 4194304 - hp * 512 - h_ * 262144, 0x00020000);
      const unsigned v0 = ((lane >> 2) & 3) * 8192 + (lane >> 4) * 64 + (lane & 3) * 16, v1 = v0 + 31744, v2 = (v0 ^ 32) + 2 * 31744, v3 = v2 + 31744;
      dma4(r_, v0, v1, v2, v3, c_ * 524288u, lds0 + wid * 8192); dma4(r_, v0, v1, v2, v3, c_ * 524288u + 131072u, lds0 + wid * 8192 + 4096);
    }
  }
  u64 tag;
  {
    const u64 pc = (pc0 & ~255ull) + (u64)min((unsigned)(idx * 4 + wid) * 384u, CODE_BYTES - 384u);
    unsigned junk;
    asm volatile("s_load_dwordx2 %0, %2, 0x0\n\ts_load_dword %1, %3, 0x0\n\ts_load_dword %1, %3, 0x40\n\ts_load_dword %1, %3, 0x80\n\t"
                 "s_load_dword %1, %3, 0xc0\n\ts_load_dword %1, %3, 0x100\n\ts_load_dword %1, %3, 0x140\n\ts_waitcnt lgkmcnt(0)"
                 : "=&s"(tag), "=&s"(junk) : "s"(WB + WS_E), "s"(pc) : "memory"); }
  tag = next_tag(tag);
#define RNEXT(a) (((a) + PAIR) & (RING - 1))
#define WBAR() do { __builtin_amdgcn_s_waitcnt(0xC07F); BAR(); } while (0)
#define KRD(kt, OFF, i) lds_b128((kt) + ((OFF) + 4096 * ((i) & 3)))
#define VRD(OFF, i, T) lds_tr((((i) & 1) ? ca1 : ca0) + ((OFF) + 8192 * ((i) >> 3) + 4096 * (T) + 256 * (((i) & 7) >> 1)))
#define WRD(i, T) lds_tr((((i) & 1) ? ca1 : ca0) + (8192 * ((i) >> 3) + 1024 * (T) + 256 * (((i) & 7) >> 1)))
  u32x4 qf[8], kf[16];
  s16x4 vl[38], vh[38];
  unsigned ca0, ca1, ka, kt, qw[4];
  float qsc = QSCALE; PIN(qsc);
  f32x4 qa[16];
  {
    const unsigned c16 = lane & 15, g4 = lane >> 4;
    const unsigned wbase = lds0 + SLOT + g4 * 2048 + ((lane >> 2) & 3) * 64 + (g4 & 1) * 32 + (lane & 3) * 8;
    const unsigned xbase = lds0 + (wid * 32 + c16) * 128 + ((g4 ^ (c16 >> 1)) * 16);
    f32x4 acc[32];
    { u32x4 z4 = {0u, 0u, 0u, 0u}; PIN(z4); NOPS(1);
#pragma unroll
      for (int d = 0; d < 32; ++d) MZ(acc[d], z4, z4); }
    u32x4 xa_[4], xb_[4];

    waitvm<0>(); BAR();

    __builtin_amdgcn_s_waitcnt(0xC07F);
    unsigned uo = 0, xa = xbase; ca0 = wbase; ca1 = ca0 ^ 32;
    xa_[1] = lds_b128(xa + 2048); xa_[0] = lds_b128(xa); SBAR();
#pragma unroll
    for (int i = 0; i < 6; ++i) { vl[i ^ 1] = WRD(i ^ 1, 0); vh[i ^ 1] = WRD(i ^ 1, 1); SBAR(); }
    xa_[3] = lds_b128((xa ^ 64) + 2048); xa_[2] = lds_b128(xa ^ 64);
#define QUNIT(XC, XN, FIRST, LAST)                                                                                      \
    do {                                                                                                                \
      SBAR();                                                                                                           \
      _Pragma("unroll") for (int i = 0; i < 32; ++i) {                                                                  \
        const int ai = (i >> 4) * 16 + (i & 7) * 2, xi = ((i >> 3) & 1) * 2, j = (i ^ 1) + 6, n_ = 2 * (i - 28);        \
        MA(acc[ai], CAT(vl[i], vh[i]), XC[xi]); SBAR();                                                                 \
        if (i == 9) { if (FIRST) BAR(); }                                                                               \
        if (i == 28) WBAR();                                                                                            \
        if (j < 32) vl[j] = WRD(j, 0);                                                                                  \
        else if (i >= 28 && i < 31 && !(LAST)) { vl[33 + n_] = WRD(n_ + 1, 0); vh[33 + n_] = WRD(n_ + 1, 1); }          \
        else if (i >= 28 && (LAST)) kf[(i - 28) ^ 1] = KRD(ka, 0, (i - 28) ^ 1);                                        \
        if (i == 31 && !(LAST)) { XN[3] = lds_b128((xa ^ 64) + 2048); XN[2] = lds_b128(xa ^ 64); }                      \
        if (i == 26) { if (!(LAST)) { uo = (uo == 6 * SLOT) ? 0u : uo + 3 * SLOT; xa = xbase + uo; ca0 = wbase + uo; ca1 = ca0 ^ 32; PIN(xa); PIN(ca0); PIN(ca1); } \
                       else { const unsigned l_ = lane_id(); ka = lds0 + 3 * PAIR + (l_ & 15) * 256 + (((l_ >> 4) ^ (l_ & 15)) * 16); PIN(ka); } }        \
        if ((LAST) && i >= 17 && i < 25) { const int f_ = i - 17; SPACK(qw[0], qw[1], acc[(f_ >> 1) * 4 + (f_ & 1)], qsc); }                          \
        if ((LAST) && i >= 25 && i < 29) { NOPS(0); qf[2 * (i - 25)] = natural(qf[2 * (i - 25)]); PIN(qf[2 * (i - 25)]); }                             \
        SBAR();                                                                                                         \
        MA(acc[ai + 1], CAT(vl[i], vh[i]), XC[xi + 1]); SBAR();                                                         \
        if (j < 32) vh[j] = WRD(j, 1);                                                                                  \
        else if (i >= 28 && i < 31 && !(LAST)) { if (i == 28) { XN[1] = lds_b128(xa + 2048); XN[0] = lds_b128(xa); } vl[32 + n_] = WRD(n_, 0); vh[32 + n_] = WRD(n_, 1); } \
        if ((LAST) && i >= 17 && i < 25) { const int f_ = i - 17; SPACK(qw[2], qw[3], acc[(f_ >> 1) * 4 + 2 + (f_ & 1)], qsc); qf[f_] = u32x4{qw[0], qw[1], qw[2], qw[3]}; } \
        if ((LAST) && i >= 25 && i < 29) { qf[2 * (i - 25) + 1] = natural(qf[2 * (i - 25) + 1]); PIN(qf[2 * (i - 25) + 1]); }                          \
        SBAR();                                                                                                         \
      }                                                                                                                 \
      if (!(LAST)) { _Pragma("unroll") for (int i = 0; i < 6; ++i) { vl[i] = vl[32 + i]; vh[i] = vh[32 + i]; } }        \
    } while (0)

#pragma unroll 1
    for (int D = 0; D < 3; ++D) { QUNIT(xa_, xb_, D == 0, false); QUNIT(xb_, xa_, false, false); }

    QUNIT(xa_, xb_, false, false); QUNIT(xb_, xa_, false, true);

#pragma unroll
    for (int d = 0; d < 16; ++d) qa[d] = acc[16 + d];
  }

  { const unsigned l_ = lane_id(); ca0 = lds0 + 3 * PAIR + (l_ >> 4) * 1024 + ((l_ >> 2) & 3) * 64 + ((l_ >> 4) & 1) * 32 + (l_ & 3) * 8; ca1 = ca0 ^ 32; }
  u32x4 ones = {0x3F803F80u, 0x3F803F80u, 0x3F803F80u, 0x3F803F80u};
  PIN(ones);
  const rsrc_t rso = __builtin_amdgcn_make_buffer_rsrc((void*)(WB + WS_O + (gj * 8) * 32768 + wid * 8192), 0, 8 * 32768 - wid * 8192, 0x00020000);
  char* const fbase = WB + WS_F + wid * 64;
  u32x4 of2[8], oa[8], ob[8], oc[8];
  f32x4 sa[8], sb[8];
#define OLD1(dst, hl, i_) dst[i_] = __builtin_amdgcn_raw_buffer_load_b128(rso, vo + ((i_) & 3) * 1024, (unsigned)(hl) * 32768u + ((i_) >> 2) * 4096, SC1)
#define ST(e) ((((e) >> 4) * 2 + (((e) >> 2) & 1)) * 2 + (((e) >> 3) & 1))
#define SV(C, e) C[ST(e)][(e) & 3]
#define EXPV(C, e) do { float t_ = SV(C, e); asm volatile("v_exp_f32_e32 %0, %0" : "+v"(t_)); SV(C, e) = t_; } while (0)
#define PACKP(C, q) do { unsigned t_; asm volatile("v_cvt_pk_bf16_f32 %0, %1, %2" : "=v"(t_) : "v"(SV(C, 2 * (q))), "v"(SV(C, 2 * (q) + 1))); pw[(q) >> 2][(q) & 3] = t_; } while (0)
#define TILE0(PARK)                                                                                                     \
  do { const unsigned pa = lds0 + L_PARK + wid * 8192 + lane_id() * 16;                                                 \
    kt = ka ^ 64; PIN(kt); NOPS(1);                                                                                     \
    SBAR();                                                                                                             \
    _Pragma("unroll") for (int i = 0; i < 16; ++i) { const int kb = i & 3, s_ = i >> 2, j = (i ^ 1) + 4, f_ = i >> 1;   \
      if (s_ == 0) MZ(sa[kb * 2], kf[i], qf[0]); else MA(sa[kb * 2], kf[i], qf[s_ * 2]);                                \
      SBAR();                                                                                                           \
      if (PARK) { SPACK(qw[(i & 1) * 2], qw[(i & 1) * 2 + 1], qa[(f_ >> 1) * 4 + (i & 1) * 2 + (f_ & 1)], qsc);         \
                  if (i & 1) of2[f_] = u32x4{qw[0], qw[1], qw[2], qw[3]}; }                                             \
      SBAR();                                                                                                           \
      if (s_ == 0) MZ(sa[kb * 2 + 1], kf[i], qf[1]); else MA(sa[kb * 2 + 1], kf[i], qf[s_ * 2 + 1]);                    \
      SBAR();                                                                                                           \
      if (j < 16) kf[j] = KRD(kt, 0, j); else kf[j - 16] = KRD(ka, SLOT, j - 16);                                       \
      if (i == 3 || i == 7) { kt = ka ^ (64 * ((i + 5) >> 2)); PIN(kt); }                                               \
      SBAR();                                                                                                           \
      if ((PARK) && (i & 1)) { of2[f_] = natural(of2[f_]); PIN(of2[f_]); SBAR(); }                                      \
    }                                                                                                                   \
    kt = ka ^ 64; PIN(kt); kf[7] = KRD(kt, SLOT, 7); kf[6] = KRD(kt, SLOT, 6); SBAR();                                                \
                 \
    _Pragma("unroll") for (int i = 0; i < 8; ++i) *(LAS u32x4*)(size_t)(pa + i * 1024) = of2[i];                        \
    SBAR();                                                                                                             \
  } while (0)
#define PINO() do { _Pragma("unroll") for (int d_ = 0; d_ < 16; ++d_) PIN(o[d_]); PIN(ls[0]); PIN(ls[1]); } while (0)
#define TMAX(C, qb) allmax4(fmaxf(max3f(max3f(max3f(C[qb][0], C[qb][1], C[qb][2]), C[qb][3], C[2 + qb][0]), C[2 + qb][1], max3f(C[2 + qb][2], C[2 + qb][3], C[4 + qb][0])), \
                                  max3f(max3f(C[4 + qb][1], C[4 + qb][2], C[4 + qb][3]), max3f(C[6 + qb][0], C[6 + qb][1], C[6 + qb][2]), C[6 + qb][3])))
  f32x4 o[16], ls[2];
  TILE0(true);
#pragma unroll
  for (int hd = 0; hd < 2; ++hd) {
    f32x4 negm[2];
    u32x4 pw[4];
    float fct0 = 1.f, fct1 = 1.f;
    bool pend = false;
    { u32x4 z4 = {0u, 0u, 0u, 0u}; PIN(z4); NOPS(1);
      MZ(ls[0], z4, z4); MZ(ls[1], z4, z4); NOPS(5);
#pragma unroll
      for (int t = 0; t < 8; ++t) PIN(sa[t]);
      float m0 = TMAX(sa, 0), m1 = TMAX(sa, 1);
      PIN(m0); PIN(m1);
      SBAR();
#pragma unroll
      for (int t = 0; t < 16; ++t) {
        MZ(o[t], z4, z4); SBAR();
        { float v0 = sa[t >> 1][(t & 1) * 2], v1 = sa[t >> 1][(t & 1) * 2 + 1]; const float m_ = (t & 2) ? m1 : m0;
          if (t < 8) asm volatile("v_sub_f32_e32 %0, %0, %2\n\tv_sub_f32_e32 %1, %1, %2\n\tv_exp_f32_e32 %0, %0\n\tv_exp_f32_e32 %1, %1" : "+v"(v0), "+v"(v1) : "v"(m_));
          else asm volatile("v_sub_f32_e32 %0, %0, %2\n\tv_sub_f32_e32 %1, %1, %2" : "+v"(v0), "+v"(v1) : "v"(m_));
          sa[t >> 1][(t & 1) * 2] = v0; sa[t >> 1][(t & 1) * 2 + 1] = v1; }
        SBAR();
      }
#pragma unroll
      for (int r = 0; r < 4; ++r) { negm[0][r] = -m0; negm[1][r] = -m1; }
      PIN(negm[0]); PIN(negm[1]); NOPS(1);
    }
#define MX2(m, a, b) asm volatile("v_max3_f32 %0, %0, %1, %2" : "+v"(m) : "v"(a), "v"(b))
#define STEP(C, P, LAST)                                                                                                \
    do {                                                                                                                \
      SBAR();                                                                                                           \
      _Pragma("unroll") for (int i = 0; i < 16; ++i) { const int kb = i & 3, s_ = i >> 2, j = ((i + 2) ^ 3) + 4;        \
        if (s_ == 0) MC(C[kb * 2], kf[i], qf[0], negm[0]); else MA(C[kb * 2], kf[i], qf[s_ * 2]);                       \
        SBAR();                                                                                                         \
        if (i == 11) WBAR();                                                                                            \
        if (j < 16) kf[j] = KRD(kt, SLOT, j);                                                                           \
        else if (i == 11 || i == 12) { vl[25 - 2 * i] = VRD(0, 25 - 2 * i, 0); vh[25 - 2 * i] = VRD(0, 25 - 2 * i, 1); } \
        else if (i >= 14) vl[j - 16] = VRD(0, j - 16, 0);                                                               \
        SBAR();                                                                                                         \
        if (i < 4) EXPV(P, 16 + 2 * i); else if (i < 10) PACKP(P, i - 4);                                               \
        if (i == 10) { ca0 = RNEXT(ca0); PIN(ca0); PACKP(P, 6); }                                                       \
        if (i == 13) EXPV(P, 31);                                                                                       \
        if (i == 14) PACKP(P, 8);                                                                                       \
        if (i == 15) PACKP(P, 10);                                                                                      \
        SBAR();                                                                                                         \
        if (s_ == 0) MC(C[kb * 2 + 1], kf[i], qf[1], negm[1]); else MA(C[kb * 2 + 1], kf[i], qf[s_ * 2 + 1]);           \
        SBAR();                                                                                                         \
        if (i == 11 || i == 12) { vl[24 - 2 * i] = VRD(0, 24 - 2 * i, 0); vh[24 - 2 * i] = VRD(0, 24 - 2 * i, 1); }     \
        else if (i >= 14) vh[j - 16] = VRD(0, j - 16, 1);                                                               \
        SBAR();                                                                                                         \
        if (i < 4) EXPV(P, 17 + 2 * i); else if (i < 10) EXPV(P, 20 + i);                                               \
        if (i == 10) { ca1 = ca0 ^ 32; PIN(ca1); EXPV(P, 30); }                                                         \
        if (i == 13) { ka = RNEXT(ka); PIN(ka); PACKP(P, 7); }                                                          \
        if (i == 14) PACKP(P, 9);                                                                                       \
        if (i == 1 || i == 5) { kt = ka ^ (64 * ((i + 7) >> 2)); PIN(kt); }                                             \
        SBAR();                                                                                                         \
      }                                                                                                                 \
      float m0 = THR, m1 = THR;                                                                                         \
      _Pragma("unroll") for (int i = 0; i < 16; ++i) { const int e_ = i & 7, u_ = i >> 3, j = ((i + 2) ^ 3) + 4;        \
        MA(o[e_ * 2], CAT(vl[i], vh[i]), pw[u_ * 2]);                                                                   \
        SBAR();                                                                                                         \
        if (j < 16) vl[j] = VRD(0, j, 0);                                                                               \
        else if (!(LAST)) kf[j - 16] = KRD(j < 20 ? ka : kt, SLOT, j - 16);                                             \
        SBAR();                                                                                                         \
        if (i == 0) PACKP(P, 11);                                                                                       \
        if (i >= 1 && i <= 4) { MX2(m0, C[2 * i - 2][0], C[2 * i - 2][1]); MX2(m1, C[2 * i - 2][2], C[2 * i - 2][3]); } \
        if (i == 5) PACKP(P, 15);                                                                                       \
        if (i >= 6) { const int x_ = XB(2 * i - 12); if (x_ >= 0) EXPV(C, x_); }                                        \
        if ((LAST) && i >= 12) EXPV(C, 16 + (i - 12));                                                                  \
        SBAR();                                                                                                         \
        MA(o[e_ * 2 + 1], CAT(vl[i], vh[i]), pw[u_ * 2 + 1]);                                                           \
        SBAR();                                                                                                         \
        if (j < 16) vh[j] = VRD(0, j, 1);                                                                               \
        SBAR();                                                                                                         \
        if (i == 0) PACKP(P, 12);                                                                                       \
        if (i >= 1 && i <= 4) { MX2(m0, C[2 * i - 1][0], C[2 * i - 1][1]); MX2(m1, C[2 * i - 1][2], C[2 * i - 1][3]); } \
        if (i == 5) {                                                                                                   \
          if (__builtin_expect(__any(m0 != m1 || m0 != THR), 0)) {        \
            const float d0 = fmaxf(TMAX(C, 0), 0.f), d1 = fmaxf(TMAX(C, 1), 0.f), a1 = lane_id() < 16u ? 1.f : 0.f;     \
            fct0 = EXP2(-d0); fct1 = EXP2(-d1); pend = true;                                                            \
            add_col5(C[0], C[2], C[4], C[6], negm[0], a1, -d0); add_col5(C[1], C[3], C[5], C[7], negm[1], a1, -d1);     \
          } }                                                                                                           \
        if (i >= 6) { const int x_ = XB(2 * i - 11); if (x_ >= 0) EXPV(C, x_); }                                        \
        if (i == 12 && !(LAST)) { kt = ka ^ 64; PIN(kt); }                                                              \
        SBAR();                                                                                                         \
        if ((i & 3) == 3) { MA(ls[(i >> 2) & 1], ones, pw[i >> 2]); SBAR();                                             \
          if (i == 3) { PACKP(P, 13); PACKP(P, 14); } else EXPV(C, 13 + ((i >> 2) - 1)); SBAR(); }                      \
      }                                                                                                                 \
      if (__builtin_expect(pend, 0)) { NOPS(7); PINO();                                                                 \
        _Pragma("unroll") for (int d = 0; d < 16; ++d) _Pragma("unroll") for (int e = 0; e < 4; ++e) o[d][e] *= (d & 1) ? fct1 : fct0;  \
        ls[0][0] *= fct0; ls[1][0] *= fct1; pend = false;                                                               \
        PINO(); NOPS(1); }                                                                                              \
    } while (0)
#define XB(h) ((h) < 6 ? (h) : (h) == 7 ? 6 : ((h) & 1) ? 7 + ((h) - 9) / 2 : -1)
#pragma unroll 1
    for (int t = 1; t < 7; t += 2) {
      STEP(sb, sa, false);
      STEP(sa, sb, false);
    }
    STEP(sb, sa, true);
    {
#pragma unroll
      for (int e = 20; e < 32; ++e) EXPV(sb, e);
#pragma unroll
      for (int q = 0; q < 16; ++q) PACKP(sb, q);
      NOPS(1);
      MA(ls[0], ones, pw[0]); MA(ls[1], ones, pw[1]); MA(ls[0], ones, pw[2]); MA(ls[1], ones, pw[3]);
      const unsigned pa_ = lds0 + L_PARK + wid * 8192 + lane_id() * 16;
#pragma unroll
      for (int i = 0; i < 8; ++i) oa[i] = lds_b128(pa_ + i * 1024);
      NOPS(7); PIN(ls[0]); PIN(ls[1]);
      float inv0 = __builtin_amdgcn_rcpf(ls[0][0]), inv1 = __builtin_amdgcn_rcpf(ls[1][0]);
      PIN(inv0); PIN(inv1);
      WBAR();
      unsigned w4[4], tmp_;
      const unsigned vo = lane_id() * 16;
#define DF(i) ((((i) >> 1) & 1) * 8 + ((i) >> 2) * 2 + ((i) & 1))
#pragma unroll
      for (int i = 0; i < 6; ++i) { vl[i ^ 1] = VRD(SLOT, DF(i ^ 1), 0); vh[i ^ 1] = VRD(SLOT, DF(i ^ 1), 1); SBAR(); }
      SBAR();
#pragma unroll
      for (int i = 0; i < 22; ++i) { const int e_ = DF(i) & 7, u_ = (i >> 1) & 1, j = (i ^ 1) + 6, f_ = ((i - 6) >> 2) * 2 + (((i - 6) >> 1) & 1);
        if (i < 16) { MA(o[e_ * 2], CAT(vl[i], vh[i]), pw[u_ * 2]); SBAR(); }
        if (i == 18) NOPS(3);
        if (j < 16) vl[j] = VRD(SLOT, DF(j), 0);
        const float sc = (f_ & 1) ? inv1 : inv0;
        if (i >= 6) { const f32x4& t_ = o[(f_ >> 1) * 4 + (i & 1) * 2 + (f_ & 1)];
          asm volatile("v_mul_f32_e32 %0, %2, %3\n\tv_mul_f32_e32 %1, %2, %4\n\tv_cvt_pk_bf16_f32 %0, %0, %1" : "=&v"(w4[(i & 1) * 2]), "=&v"(tmp_) : "v"(sc), "v"(t_[0]), "v"(t_[1])); }
        if (hd == 1 && i < 8) OLD1(ob, 2 * ((gr + 1) & 3), i & 7);
        SBAR();
        if (i < 16) { MA(o[e_ * 2 + 1], CAT(vl[i], vh[i]), pw[u_ * 2 + 1]); SBAR(); }
        if (j < 16) vh[j] = VRD(SLOT, DF(j), 1);
        if (i >= 6) { const f32x4& t_ = o[(f_ >> 1) * 4 + (i & 1) * 2 + (f_ & 1)];
          asm volatile("v_mul_f32_e32 %0, %2, %3\n\tv_mul_f32_e32 %1, %2, %4\n\tv_cvt_pk_bf16_f32 %0, %0, %1" : "=&v"(w4[(i & 1) * 2 + 1]), "=&v"(tmp_) : "v"(sc), "v"(t_[2]), "v"(t_[3]));
          if (i & 1) of2[f_] = u32x4{w4[0], w4[1], w4[2], w4[3]}; }
        if (hd == 1 && i >= 10 && i < 18) OLD1(oc, 2 * ((gr + 2) & 3), (i - 10) & 7);
        if (hd == 1 && i >= 11 && (i & 1)) { if (i == 11) { *(LAS u32x4*)(size_t)(pa_) = of2[0]; *(LAS u32x4*)(size_t)(pa_ + 1024) = of2[1]; } *(LAS u32x4*)(size_t)(pa_ + f_ * 1024) = of2[f_]; }
        SBAR();
      }
      if (hd == 0) {
#pragma unroll
      for (int i = 0; i < 8; ++i) qf[i] = oa[i];
      WBAR();
      ka = RNEXT(ka); ca0 = RNEXT(ca0); ca1 = ca0 ^ 32;
      kf[1] = KRD(ka, 0, 1); kf[0] = KRD(ka, 0, 0); kf[3] = KRD(ka, 0, 3); kf[2] = KRD(ka, 0, 2);

      TILE0(false);
      }
    }
  }

  {
    const unsigned l_ = lane_id(), vo = l_ * 16;
    f32x4 acc[16];
#define FIDX(NC, i) ((NC) == 8 ? (i) : (NC) == 6 ? ((i) / 6) * 8 + 2 + (i) % 6 : (((i) >> 1) & 3) * 8 + 2 + 2 * ((i) >> 3) + ((i) & 1))
#define FCNT(NC) ((NC) == 8 ? 32 : 24)
#define OPAIR(B, FIRST, NOBAR, NC, NN, MID)                                                                                    \
    do {                                                                                                                \
      SBAR();                                                                                                           \
      _Pragma("unroll") for (int i = 0; i < FCNT(NC); ++i) {                                                            \
        const int f_ = FIDX(NC, i), N_ = FCNT(NC), j = (i ^ 1) + 6, n_ = 2 * (i - (N_ - 4));                            \
        if ((FIRST) && i < 8) MZ(acc[(f_ & 7) * 2], CAT(vl[i], vh[i]), B[0]); else MA(acc[(f_ & 7) * 2], CAT(vl[i], vh[i]), B[(f_ >> 3) * 2]); \
        SBAR();                                                                                                         \
        if ((NN) && i == N_ - 4 && !(NOBAR)) WBAR();                                                                    \
        if (j < N_) vl[j] = VRD(0, FIDX(NC, j), 0);                                                                     \
        else if ((NN) && i >= N_ - 4 && i < N_ - 1) { vl[N_ + n_ + 1] = VRD(0, FIDX(NN, n_ + 1), 0); vh[N_ + n_ + 1] = VRD(0, FIDX(NN, n_ + 1), 1); } \
        if ((NN) && i == N_ - 6) { ca0 = RNEXT(ca0); ca1 = ca0 ^ 32; PIN(ca0); PIN(ca1); }                              \
        { MID; }                                                                                                        \
        SBAR();                                                                                                         \
        if ((FIRST) && i < 8) MZ(acc[(f_ & 7) * 2 + 1], CAT(vl[i], vh[i]), B[1]); else MA(acc[(f_ & 7) * 2 + 1], CAT(vl[i], vh[i]), B[(f_ >> 3) * 2 + 1]); \
        SBAR();                                                                                                         \
        if (j < N_) vh[j] = VRD(0, FIDX(NC, j), 1);                                                                     \
        else if ((NN) && i >= N_ - 4 && i < N_ - 1) { vl[N_ + n_] = VRD(0, FIDX(NN, n_), 0); vh[N_ + n_] = VRD(0, FIDX(NN, n_), 1); } \
        SBAR();                                                                                                         \
      }                                                                                                                 \
      if (NN) { _Pragma("unroll") for (int i = 0; i < 6; ++i) { vl[i] = vl[FCNT(NC) + i]; vh[i] = vh[FCNT(NC) + i]; } } \
    } while (0)
    WBAR();
    ca0 = RNEXT(ca0); ca1 = ca0 ^ 32;
#pragma unroll
    for (int i = 0; i < 6; ++i) { vl[i ^ 1] = VRD(0, i ^ 1, 0); vh[i ^ 1] = VRD(0, i ^ 1, 1); SBAR(); }
#define OFET(dst, hl, i0, st) if (i >= (i0) && (i - (i0)) % (st) == 0 && (i - (i0)) / (st) < 8) OLD1(dst, hl, ((i - (i0)) / (st)) & 7)
    OPAIR(of2, true, true, 8, 8, {});
    OPAIR(oa, false, false, 8, 8, {});
    OPAIR(ob, false, false, 8, 8, { OFET(of2, 2 * ((gr + 3) & 3), 12, 2); });

    OPAIR(oc, false, false, 8, 6, { OFET(oa, 2 * ((gr + 1) & 3) + 1, 12, 2); });

    OPAIR(of2, false, false, 6, 6, { OFET(ob, 2 * ((gr + 2) & 3) + 1, 10, 1); });
    OPAIR(oa, false, false, 6, 6, { OFET(oc, 2 * ((gr + 3) & 3) + 1, 10, 1); });
    const rsrc_t rp = __builtin_amdgcn_make_buffer_rsrc((void*)(WB + WS_P + gr * 131072 + wid * 2048), 0, 131072 - wid * 2048, 0x00020000);
#define PSEND(s, qb) do { PIN(acc[4 * (s) + (qb)]); PIN(acc[4 * (s) + 2 + (qb)]);                                       \
      __builtin_amdgcn_raw_buffer_store_b128(pack2n(acc[4 * (s) + (qb)], acc[4 * (s) + 2 + (qb)]), rp,                   \
                                             vo + (((unsigned)(gj + (s)) & 3u) * 4u + (unsigned)gj) * 8192u + (qb) * 1024, 0, SC1); } while (0)
    const char* const pflag = fbase + (4 * ((gj + 1 + (l_ & 3) % 3) & 3) + gr) * 768 + 512;
    OPAIR(ob, false, false, 6, 7, {});
    OPAIR(oc, false, false, 7, 0, { if (i == 11 || i == 12) PSEND(1, (i - 11) & 1); if (i == 20 || i == 21) PSEND(2, (i - 20) & 1); });

    u64 fv;
    { unsigned c4[4];
      c4[3] = ca0; c4[0] = RNEXT(ca0); c4[1] = RNEXT(c4[0]); c4[2] = RNEXT(c4[1]);
#define DRD(i, T) lds_tr((c4[(i) >> 3] ^ (((i) & 1) * 32)) + (8192 * (((i) >> 1) & 3) + 4096 * (T)))
#pragma unroll
      for (int i = 0; i < 4; ++i) { vl[i ^ 1] = DRD(i ^ 1, 0); vh[i ^ 1] = DRD(i ^ 1, 1); SBAR(); }
      PSEND(3, 0); PSEND(3, 1);
      SBAR();
#pragma unroll
      for (int i = 4; i < 6; ++i) { vl[i ^ 1] = DRD(i ^ 1, 0); vh[i ^ 1] = DRD(i ^ 1, 1); SBAR(); }
#pragma unroll
      for (int i = 0; i < 32; ++i) { const int j = (i ^ 1) + 6, k_ = ((i >> 1) & 3) * 2;
        const u32x4 b0 = (i >> 3) == 0 ? of2[k_] : (i >> 3) == 1 ? oa[k_] : (i >> 3) == 2 ? ob[k_] : oc[k_];
        const u32x4 b1 = (i >> 3) == 0 ? of2[k_ + 1] : (i >> 3) == 1 ? oa[k_ + 1] : (i >> 3) == 2 ? ob[k_ + 1] : oc[k_ + 1];
        MA(acc[(i & 1) * 2], CAT(vl[i], vh[i]), b0);
        SBAR();
        if (j < 32) vl[j] = DRD(j, 0);
        if (i == 16) { waitvm<0>(); if (l_ == 0) gst(fbase + hp * 768 + 512, tag); fv = gld(pflag); }
        SBAR();
        MA(acc[(i & 1) * 2 + 1], CAT(vl[i], vh[i]), b1);
        SBAR();
        if (j < 32) vh[j] = DRD(j, 1);
        SBAR();
      }
    }
    if (!__all(fv == tag)) gwait(pflag, tag);
    asm volatile("" ::: "memory");

    const rsrc_t rout = __builtin_amdgcn_make_buffer_rsrc((void*)(OUT + ((size_t)(b * 128 + wid * 32) * 512 + gr * 128 + gj * 32) * 2), 0, 32768 - (gr * 128 + gj * 32) * 2, 0x00020000);
    u32x4 pp[3][2];
#pragma unroll
    for (int s = 0; s < 3; ++s) {
      const unsigned src = (unsigned)(gj + 1 + s) & 3u;
      pp[s][0] = __builtin_amdgcn_raw_buffer_load_b128(rp, vo, ((unsigned)gj * 4 + src) * 8192u, SC1);
      pp[s][1] = __builtin_amdgcn_raw_buffer_load_b128(rp, vo + 1024, ((unsigned)gj * 4 + src) * 8192u, SC1);
    }
    if (wid == 0 && hp == 0 && l_ == 0) gst(WB + WS_E, tag);
#pragma unroll
    for (int qb = 0; qb < 2; ++qb) {
#pragma unroll
      for (int s = 0; s < 3; ++s)
#pragma unroll
        for (int w = 0; w < 4; ++w) {
          const unsigned x = pp[s][qb][w];
          acc[(w >> 1) * 2 + qb][(w & 1) * 2] += __uint_as_float(x << 16); acc[(w >> 1) * 2 + qb][(w & 1) * 2 + 1] += __uint_as_float(x & 0xffff0000u);
        }
      __builtin_amdgcn_raw_buffer_store_b128(natural(pack2n(acc[qb], acc[2 + qb])), rout, (qb * 16 + (l_ & 15)) * 1024 + (l_ >> 4) * 16, 0, SC1);
    }
  }

}

extern "C" void kernel_launch(void* const* d_in, const int* in_sizes, int n_in, void* d_out, int out_size, void* d_ws, size_t ws_size, hipStream_t stream) {
  const char* x = (const char*)d_in[0]; const char* k = (const char*)d_in[1]; const char* v = (const char*)d_in[2];
  const char* wq = (const char*)d_in[3]; const char* wo = (const char*)d_in[4];
  (void)hipFuncSetAttribute((const void*)mha_fused, hipFuncAttributeMaxDynamicSharedMemorySize, (int)L_END);
  mha_fused<<<256, 384, L_END, stream>>>(x, k, v, wq, wo, (char*)d_ws, (char*)d_out);
}
```
